```python
import torch

def _softmax(x, axis=-1):
    return torch.softmax(x, dim=axis)
def _gelu(x):
    return torch.nn.functional.gelu(x, approximate="tanh")
def _silu(x):
    return torch.nn.functional.silu(x)
def _sigmoid(x):
    return torch.sigmoid(x)
def _layernorm(x, axis=-1, eps=1e-5):
    m = x.mean(dim=axis, keepdim=True)
    v = x.var(dim=axis, keepdim=True, unbiased=False)
    return (x - m) / torch.sqrt(v + eps)
def _scatter_add(tgt, idx, src, axis=0):
    return tgt.clone().index_add_(axis, idx, src)
def _segment_sum(src, seg, n):
    out = torch.zeros((n,) + src.shape[1:], dtype=src.dtype)
    return out.index_add_(0, seg, src)
def _pad(x, pw):
    flat = tuple(v for pair in reversed(pw) for v in pair)
    return torch.nn.functional.pad(x, flat)

kernel_name = 'sequential_preLN__ssm_conv_s4d__glu_gated_geglu__prompt'

def setup_inputs():

    _w = (lambda a: (lambda t: t.to(torch.bfloat16) if t.is_floating_point() else t)(torch.as_tensor(a)))
    def _A_setup():
        Bsz, L, D, N = 1, 2048, 640, 192
        return {
            "u": _w((lambda *s: torch.randn(*s, dtype=torch.bfloat16) * 0.05)(Bsz, L, D)),
            "log_A_real": _w((lambda *s: torch.randn(*s, dtype=torch.bfloat16) * 6.4)(D, N) * 0.5),
            "A_imag": _w((lambda *s: torch.randn(*s, dtype=torch.bfloat16) * 0.05)(D, N) * 3.14),
            "C_real": _w((lambda *s: torch.randn(*s, dtype=torch.bfloat16) * 0.05)(D, N) / N ** 0.5),
            "C_imag": _w((lambda *s: torch.randn(*s, dtype=torch.bfloat16) * 0.05)(D, N) / N ** 0.5),
            "log_dt": _w((lambda *s: torch.randn(*s, dtype=torch.bfloat16) * 0.05)(D) * 0.5 - 4.0),
            "D_skip": _w((lambda *s: torch.randn(*s, dtype=torch.bfloat16) * 0.05)(D)),
        }
    _A_in = _A_setup()

    def _B_setup():
        B, S, Dm, Df = 1, 2048, 640, 1536
        return {
            "x": _w((lambda *s: torch.randn(*s, dtype=torch.bfloat16) * 0.05)(B, S, Dm)),
            "w_gate": _w((lambda *s: torch.randn(*s, dtype=torch.bfloat16) * 0.4)(Dm, Df) * (Dm ** -0.5)),
            "w_up": _w((lambda *s: torch.randn(*s, dtype=torch.bfloat16) * 0.4)(Dm, Df) * (Dm ** -0.5)),
            "w_down": _w((lambda *s: torch.randn(*s, dtype=torch.bfloat16) * 0.4)(Df, Dm) * (Df ** -0.5)),
        }
    _B_in = _B_setup()

    return {
        "A_u": _A_in["u"],
        "A_log_A_real": _A_in["log_A_real"],
        "A_A_imag": _A_in["A_imag"],
        "A_C_real": _A_in["C_real"],
        "A_C_imag": _A_in["C_imag"],
        "A_log_dt": _A_in["log_dt"],
        "A_D_skip": _A_in["D_skip"],
        "B_x": _B_in["x"],
        "B_w_gate": _B_in["w_gate"],
        "B_w_up": _B_in["w_up"],
        "B_w_down": _B_in["w_down"],
    }

def reference(A_u, A_log_A_real, A_A_imag, A_C_real, A_C_imag, A_log_dt, A_D_skip, B_x, B_w_gate, B_w_up, B_w_down):
    def _A_ref(u, log_A_real, A_imag, C_real, C_imag, log_dt, D_skip):
        L = u.shape[1]
        dt = torch.exp(log_dt)
        A = -torch.exp(log_A_real) + 1j * A_imag
        C = (C_real + 1j * C_imag) * dt[:, None]
        pos = (lambda x, dt: x.to(dt))(torch.arange(L), log_dt.dtype)
        P = torch.exp((A * dt[:, None])[:, :, None] * pos[None, None, :])
        K = 2.0 * torch.einsum("dn,dnl->dl", C, P).real
        L2 = 2 * L
        u_t = (lambda x, *d: x.permute(*d))(u, 0, 2, 1)
        Kf = (lambda x, *a, **k: torch.fft.rfft(x.float(), *a, **k))(K, L2)
        Uf = (lambda x, *a, **k: torch.fft.rfft(x.float(), *a, **k))(u_t, L2)
        y = (lambda x, *a, **k: torch.fft.irfft(x, *a, **k).to(torch.bfloat16))(Uf * Kf[None, :, :], L2)[:, :, :L]
        y = (lambda x, *d: x.permute(*d))(y, 0, 2, 1)
        return y + u * D_skip[None, None, :]

    def _B_ref(x, w_gate, w_up, w_down):
        g = torch.einsum("bsd,df->bsf", x, w_gate)
        u = torch.einsum("bsd,df->bsf", x, w_up)
        h = _gelu(g) * u
        return torch.einsum("bsf,fd->bsd", h, w_down)

    x = A_u
    h = x + _A_ref(_layernorm(x, axis=-1), A_log_A_real, A_A_imag, A_C_real, A_C_imag, A_log_dt, A_D_skip)
    h2 = h + _B_ref(_layernorm(h, axis=-1), B_w_gate, B_w_up, B_w_down)
    return h2
```

```cpp
#include <hip/hip_runtime.h>
#include <hip/hip_bf16.h>
#include <stdint.h>

typedef _Float16 h2v __attribute__((ext_vector_type(2)));
typedef _Float16 h4v __attribute__((ext_vector_type(4)));
typedef _Float16 h8v __attribute__((ext_vector_type(8)));
typedef __bf16 b2v __attribute__((ext_vector_type(2)));
typedef __bf16 b8v __attribute__((ext_vector_type(8)));
typedef float f2v __attribute__((ext_vector_type(2)));
typedef float f4v __attribute__((ext_vector_type(4)));
typedef float f16v __attribute__((ext_vector_type(16)));
typedef unsigned u2v __attribute__((ext_vector_type(2)));
typedef unsigned u4v __attribute__((ext_vector_type(4)));
typedef short s4v __attribute__((ext_vector_type(4)));
typedef unsigned short u16;
#define LDS_AS __attribute__((address_space(3)))
#define DEV __device__ __forceinline__

constexpr int SL = 2048;
constexpr int DM = 640;
constexpr int NS = 192;
constexpr int FF = 1536;
constexpr int KDS = 656;

constexpr size_t WS_UT = 0;
constexpr size_t WS_H = WS_UT + (size_t)SL * DM * 2;
constexpr size_t WS_HID = WS_H + (size_t)SL * DM * 2;
constexpr size_t WS_W2T = WS_HID + (size_t)SL * FF * 2;
constexpr size_t WS_KD = WS_W2T + (size_t)FF * DM * 2;
constexpr size_t WS_KS = WS_KD + (size_t)DM * KDS * 2;
constexpr size_t WS_CS = WS_KS + (size_t)DM * 4;

DEV float bf2f(unsigned v) { return __builtin_bit_cast(float, v << 16); }
DEV float bflo(unsigned v) { return __builtin_bit_cast(float, v << 16); }
DEV float bfhi(unsigned v) { return __builtin_bit_cast(float, v & 0xffff0000u); }
DEV unsigned pkbf(float a, float b) { f2v v = {a, b}; return __builtin_bit_cast(unsigned, __builtin_convertvector(v, b2v)); }
DEV unsigned pkh(float a, float b) { f2v v = {a, b}; return __builtin_bit_cast(unsigned, __builtin_convertvector(v, h2v)); }
DEV float bfr(float a) { return bflo(pkbf(a, a)); }
DEV float hlo(unsigned v) { return (float)__builtin_bit_cast(h2v, v)[0]; }
DEV float hhi(unsigned v) { return (float)__builtin_bit_cast(h2v, v)[1]; }
template <int CTRL> DEV float dppf(float v) {
  return __builtin_bit_cast(float, __builtin_amdgcn_update_dpp(0, __builtin_bit_cast(int, v), CTRL, 0xf, 0xf, false));
}
DEV float row16_sum(float v) { v += dppf<0x128>(v); v += dppf<0x124>(v); v += dppf<0x122>(v); v += dppf<0x121>(v); return v; }
DEV float row16_max(float v) { v = fmaxf(v, dppf<0x128>(v)); v = fmaxf(v, dppf<0x124>(v)); v = fmaxf(v, dppf<0x122>(v)); v = fmaxf(v, dppf<0x121>(v)); return v; }
DEV s4v trread(const char* p) { return __builtin_amdgcn_ds_read_tr16_b64_v4i16((LDS_AS s4v*)p); }
#define CFENCE() asm volatile("" ::: "memory")

struct cf { float x, y; };
DEV cf cmul(cf a, cf b) { cf r; r.x = a.x * b.x - a.y * b.y; r.y = a.x * b.y + a.y * b.x; return r; }
DEV cf csq(cf a) { cf r; r.x = a.x * a.x - a.y * a.y; r.y = 2.f * a.x * a.y; return r; }

constexpr int K1_REG = 16 * 272;
constexpr int K1_LDS = 6 * 2 * K1_REG + 64;

__global__ __launch_bounds__(384) void k1_ln_kgen(const u16* __restrict__ x, const u16* __restrict__ logA, const u16* __restrict__ Aim,
                                                  const u16* __restrict__ Cre, const u16* __restrict__ Cim, const u16* __restrict__ logdt,
                                                  char* __restrict__ ws) {
  __shared__ __attribute__((aligned(16))) char smem[K1_LDS];
  const int tid = threadIdx.x, w = tid >> 6, lane = tid & 63;
  const int blk = blockIdx.x;
  if (blk >= 320) {
    const int tb = blk - 320;
    if (w < 4) {
      const int row = 4 * w + (lane >> 4), c = lane & 15;
      const u4v* xr = (const u4v*)(x + (size_t)(16 * tb + row) * DM);
      u4v v[5];
#pragma unroll
      for (int i = 0; i < 5; ++i) v[i] = xr[c + 16 * i];
      float f[40];
      float s = 0.f;
#pragma unroll
      for (int i = 0; i < 5; ++i)
#pragma unroll
        for (int j = 0; j < 4; ++j) { f[i * 8 + 2 * j] = bflo(v[i][j]); f[i * 8 + 2 * j + 1] = bfhi(v[i][j]); s += f[i * 8 + 2 * j] + f[i * 8 + 2 * j + 1]; }
      s = row16_sum(s);
      const float mean = s * (1.f / DM);
      float q = 0.f;
#pragma unroll
      for (int i = 0; i < 40; ++i) { float dlt = f[i] - mean; q += dlt * dlt; }
      q = row16_sum(q);
      const float mb = bfr(mean), vb = bfr(q * (1.f / DM));
      const float den = bfr(sqrtf(bfr(vb + 1e-5f)));
      const float rden = 1.f / den;
#pragma unroll
      for (int i = 0; i < 5; ++i) {
        u4v o;
#pragma unroll
        for (int j = 0; j < 4; ++j) {
          unsigned t1 = pkbf(f[i * 8 + 2 * j] - mb, f[i * 8 + 2 * j + 1] - mb);
          unsigned t2 = pkbf(bflo(t1) * rden, bfhi(t1) * rden);
          o[j] = pkh(bflo(t2), bfhi(t2));
        }
        *(u4v*)(smem + row * 1312 + (c + 16 * i) * 16) = o;
      }
    }
    __syncthreads();
    if (w < 4) {
      const int G = lane >> 4, h = G & 1, q = (lane & 15) >> 2, p = lane & 3, i = lane & 15;
#pragma unroll
      for (int it = 0; it < 5; ++it) {
        const int cb = 2 * (w * 5 + it) + (G >> 1);
        const char* a = smem + (8 * h + q) * 1312 + (16 * cb + 4 * p) * 2;
        s4v r0 = trread(a), r1 = trread(a + 4 * 1312);
        u2v a0 = __builtin_bit_cast(u2v, r0), a1 = __builtin_bit_cast(u2v, r1);
        u4v o = {a0[0], a0[1], a1[0], a1[1]};
        const int ch = 16 * cb + i;
        *(u4v*)(ws + WS_UT + ((size_t)((ch >> 2) * 128 + tb) * 64 + (ch & 3) * 16 + 8 * h) * 2) = o;
      }
    }
    return;
  }
  const int cl = w / 3, wc = w - 3 * cl;
  const int d = 2 * blk + cl, n = 64 * wc + lane;
  const float dt = bfr(expf(bf2f(logdt[d])));
  const float are = -bfr(expf(bf2f(logA[d * NS + n])));
  const float zr = are * dt, zi = bf2f(Aim[d * NS + n]) * dt;
  float cr = bf2f(Cre[d * NS + n]) * dt, ci = bf2f(Cim[d * NS + n]) * dt;
  float cm = row16_max(fmaxf(fabsf(cr), fabsf(ci)));
  cm = fmaxf(cm, __shfl_xor(cm, 16)); cm = fmaxf(cm, __shfl_xor(cm, 32));
  int ex = (__builtin_bit_cast(unsigned, cm) >> 23) & 0xff;
  ex = min(max(ex, 1), 240);
  const float gs = __builtin_bit_cast(float, (unsigned)(253 - ex) << 23);
  const float gsi = __builtin_bit_cast(float, (unsigned)(ex + 1) << 23);
  const float zr2 = zr * 1.44269504088896340736f, zi2 = zi * 0.15915494309189533577f;
  auto E = [&](float p) { cf r; float m = __builtin_amdgcn_exp2f(zr2 * p); float ph = __builtin_amdgcn_fractf(zi2 * p);
                          r.x = m * __builtin_amdgcn_cosf(ph); r.y = m * __builtin_amdgcn_sinf(ph); return r; };
  char* R0 = smem + w * 2 * K1_REG;
  char* R1 = R0 + K1_REG;
  auto putF = [&](char* R, int row, cf v) { *(unsigned*)(R + row * 272 + lane * 4) = pkh(v.x, v.y); };
  auto putG = [&](char* R, int row, cf v) { *(unsigned*)(R + row * 272 + lane * 4) = pkh(v.x, -v.y); };
  const int fi = lane & 15, fg = lane >> 4;
  auto frag = [&](const char* R, int ks) { return *(const h8v*)(R + fi * 272 + ks * 64 + fg * 16); };
  {
    const cf W8 = E(8.f);
    cf F = {1.f, 0.f};
    putF(R0, 0, F);
    F = W8; putF(R0, 1, F);
#pragma unroll
    for (int b = 2; b < 16; ++b) { F = cmul(F, W8); putF(R0, b, F); }
  }
  CFENCE();
  h8v fb[4];
#pragma unroll
  for (int ks = 0; ks < 4; ++ks) fb[ks] = frag(R0, ks);
  CFENCE();
  const cf w1 = E(1.f), E128 = E(128.f);
  cf G[8];
  G[0].x = cr * gs; G[0].y = ci * gs;
#pragma unroll
  for (int i = 1; i < 8; ++i) G[i] = cmul(G[i - 1], w1);
#pragma unroll
  for (int i = 0; i < 8; ++i) { putG(R1, i, G[i]); putG(R1, 8 + i, cmul(G[i], E128)); }
  CFENCE();
  f4v acc0 = {0, 0, 0, 0}, acc1 = {0, 0, 0, 0}, acc2 = {0, 0, 0, 0};
#pragma unroll
  for (int ks = 0; ks < 4; ++ks) acc0 = __builtin_amdgcn_mfma_f32_16x16x32_f16(frag(R1, ks), fb[ks], acc0, 0, 0, 0);
  CFENCE();
  const cf E256 = csq(E128), E384 = cmul(E256, E128), E512 = csq(E256), E640 = cmul(E512, E128), E768 = cmul(E512, E256),
           E896 = cmul(E512, E384), E1024 = csq(E512);
#pragma unroll
  for (int e = 0; e < 4; ++e) { putG(R0, e, cmul(G[2 * e], E256)); putG(R0, 4 + e, cmul(G[2 * e], E384)); }
  putG(R0, 8, cmul(G[0], E512)); putG(R0, 9, cmul(G[4], E512));
  putG(R0, 10, cmul(G[0], E640)); putG(R0, 11, cmul(G[4], E640));
  putG(R0, 12, cmul(G[0], E768)); putG(R0, 13, cmul(G[4], E768));
  putG(R0, 14, cmul(G[0], E896)); putG(R0, 15, cmul(G[4], E896));
  CFENCE();
#pragma unroll
  for (int ks = 0; ks < 4; ++ks) acc1 = __builtin_amdgcn_mfma_f32_16x16x32_f16(frag(R0, ks), fb[ks], acc1, 0, 0, 0);
  CFENCE();
  {
    cf H = cmul(G[0], E1024);
    const cf H0 = H;
    putG(R1, 0, H);
#pragma unroll
    for (int a = 1; a < 8; ++a) { H = cmul(H, E128); putG(R1, a, H); }
    const cf Hl = cmul(H0, E1024);
    putG(R1, 8, Hl);
#pragma unroll
    for (int a = 9; a < 16; ++a) *(unsigned*)(R1 + a * 272 + lane * 4) = 0u;
  }
  CFENCE();
#pragma unroll
  for (int ks = 0; ks < 4; ++ks) acc2 = __builtin_amdgcn_mfma_f32_16x16x32_f16(frag(R1, ks), fb[ks], acc2, 0, 0, 0);
  CFENCE();
  {
    float* red = (float*)R0;
    const float sc = 2.f * gsi;
    const int b = fi, g = fg;
    *(f4v*)(red + 4 * (g & 1) + 128 * (g >> 1) + 8 * b) = acc0 * sc;
    if (g < 2) {
      *(f4v*)(red + 256 + 64 * g + 4 * b) = acc1 * sc;
#pragma unroll
      for (int r = 0; r < 4; ++r) red[512 + 16 * (4 * g + r) + b] = acc2[r] * sc;
    } else {
      f2v lo = {acc1[0] * sc, acc1[1] * sc}, hi = {acc1[2] * sc, acc1[3] * sc};
      *(f2v*)(red + 384 + 32 * (2 * (g - 2)) + 2 * b) = lo;
      *(f2v*)(red + 384 + 32 * (2 * (g - 2) + 1) + 2 * b) = hi;
      if (g == 2 && b == 0) red[640] = acc2[0] * sc;
    }
    if (lane == 0) ((int*)(smem + 12 * K1_REG))[w] = ex;
  }
  __syncthreads();
  {
    const int* exs = (const int*)(smem + 12 * K1_REG) + 3 * cl;
    const int em = max(exs[0], max(exs[1], exs[2]));
    const float S = __builtin_bit_cast(float, (unsigned)(em + 1 + 4) << 23);
    const float Si = __builtin_bit_cast(float, (unsigned)(253 - em - 4) << 23);
    const int tc = 64 * wc + lane;
    if (tc < KDS / 4) {
      const char* base = smem + (3 * cl) * 2 * K1_REG + tc * 16;
      f4v s;
      if (tc < 160) {
        s = *(const f4v*)(base) + *(const f4v*)(base + 2 * K1_REG) + *(const f4v*)(base + 4 * K1_REG);
      } else {
        s = f4v{0, 0, 0, 0};
        if (tc == 160) s[0] = *(const float*)(base) + *(const float*)(base + 2 * K1_REG) + *(const float*)(base + 4 * K1_REG);
      }
      s *= Si;
      u2v o = {pkh(s[0], s[1]), pkh(s[2], s[3])};
      *(u2v*)(ws + WS_KD + ((size_t)d * KDS + 4 * tc) * 2) = o;
    }
    if (tc == 0) *(float*)(ws + WS_KS + d * 4) = S;
  }
}

constexpr int K2_KC = 0;
constexpr int K2_UL = 4 * 16640;
constexpr int K2_KD = K2_UL + 4 * 4608;
constexpr int K2_XB = K2_KD + 4 * 1312;
constexpr int K2_LDS = K2_XB + 32768;

__global__ __launch_bounds__(512) void k2_conv(const u16* __restrict__ x, const u16* __restrict__ Dskip, const u16* __restrict__ Wg,
                                               const u16* __restrict__ Wu, const u16* __restrict__ Wd, char* __restrict__ ws) {
  extern __shared__ __attribute__((aligned(16))) char smem[];
  const int tid = threadIdx.x, w = tid >> 6, lane = tid & 63;
  const int blk = blockIdx.x;
  if (blk >= 208) {
    const int ks = blk - 208;
    constexpr int RS = 1280 + 32;
    for (int c = tid; c < 32 * 80; c += 512) {
      const int r = c / 80, cc = c - r * 80;
      *(u4v*)(smem + r * RS + cc * 16) = *(const u4v*)(Wd + (size_t)(32 * ks + r) * DM + cc * 8);
    }
    __syncthreads();
    const int g = lane >> 4, q = (lane >> 2) & 3, p = lane & 3;
#pragma unroll
    for (int it = 0; it < 5; ++it) {
      const int ctg = w * 5 + it;
      const char* a = smem + (16 * (g >> 1) + 4 * (g & 1) + q) * RS + (16 * ctg + 4 * p) * 2;
      u2v a0 = __builtin_bit_cast(u2v, trread(a)), a1 = __builtin_bit_cast(u2v, trread(a + 8 * RS));
      u4v o = {a0[0], a0[1], a1[0], a1[1]};
      const int cb = ctg / 5, ct = ctg - 5 * cb;
      *(u4v*)(ws + WS_W2T + ((size_t)((cb * 48 + ks) * 5 + ct) * 64 + lane) * 16) = o;
    }
    return;
  }
  if (blk >= 160) {
    const int b = blk - 160, m = b / 24, c0 = 64 * (b - 24 * m);
    const u16* W = m ? Wu : Wg;
    const int cc = tid & 7, rg = tid >> 3;
    float s[8] = {0, 0, 0, 0, 0, 0, 0, 0};
#pragma unroll
    for (int i = 0; i < 10; ++i) {
      u4v v = *(const u4v*)(W + (size_t)(rg + 64 * i) * FF + c0 + cc * 8);
#pragma unroll
      for (int j = 0; j < 4; ++j) { s[2 * j] += bflo(v[j]); s[2 * j + 1] += bfhi(v[j]); }
    }
    float* red = (float*)smem;
#pragma unroll
    for (int j = 0; j < 8; ++j) red[rg * 65 + cc * 8 + j] = s[j];
    __syncthreads();
    if (tid < 64) {
      float t = 0.f;
      for (int r = 0; r < 64; ++r) t += red[r * 65 + tid];
      *(float*)(ws + WS_CS + (size_t)(m * FF + c0 + tid) * 4) = t;
    }
    return;
  }
  const int cg = blk;
  const u4v* ut = (const u4v*)(ws + WS_UT + (size_t)cg * 16384);
  const u4v uv0 = ut[tid], uv1 = ut[tid + 512];
  u4v kv = {0, 0, 0, 0};
  if (tid < 328) kv = *(const u4v*)(ws + WS_KD + (size_t)cg * 4 * KDS * 2 + tid * 16);
  u2v xv[4];
#pragma unroll
  for (int i = 0; i < 4; ++i) xv[i] = *(const u2v*)(x + (size_t)(4 * tid + i) * DM + 4 * cg);
  const int c = w & 3, hr = w >> 2;
  const float Sc = *(const float*)(ws + WS_KS + (4 * cg + c) * 4);
  const float Dk = bf2f(Dskip[4 * cg + c]);
  if (tid < 128) *(u4v*)(smem + K2_UL + (tid >> 5) * 4608 + (tid & 31) * 16) = u4v{0, 0, 0, 0};
  if (tid >= 128 && tid < 160) { const int t = tid - 128; *(u4v*)(smem + K2_KC + (t >> 1) * 4160 + (t & 1) * 16) = u4v{0, 0, 0, 0}; }
  {
    int n = tid;
    *(u4v*)(smem + K2_UL + ((n >> 1) & 3) * 4608 + (256 + 16 * (n >> 3) + 8 * (n & 1)) * 2) = uv0;
    n = tid + 512;
    *(u4v*)(smem + K2_UL + ((n >> 1) & 3) * 4608 + (256 + 16 * (n >> 3) + 8 * (n & 1)) * 2) = uv1;
    if (tid < 328) *(u4v*)(smem + K2_KD + tid * 16) = kv;
  }
  __syncthreads();
  {
    const int tp = tid & 255, L = 8 * tp;
    int off[11];
#pragma unroll
    for (int e = 0; e < 11; ++e) {
      const int l = L - 3 + e;
      int idx = l;
      if (l >= 256) idx = (int)(pkbf((float)l, 0.f) & 0xffff) - 17024;
      if (l < 0) idx = 648;
      off[e] = idx * 2;
    }
#pragma unroll
    for (int cc = 0; cc < 2; ++cc) {
      const int ch = 2 * (tid >> 8) + cc;
      const char* kd = smem + K2_KD + ch * 1312;
      unsigned v[11];
#pragma unroll
      for (int e = 0; e < 11; ++e) v[e] = *(const u16*)(kd + off[e]);
      unsigned P[10];
#pragma unroll
      for (int e = 0; e < 10; ++e) P[e] = v[e] | (v[e + 1] << 16);
#pragma unroll
      for (int q = 0; q < 4; ++q)
        *(u4v*)(smem + K2_KC + ch * 16640 + q * 4160 + (tp + 2) * 16) = u4v{P[3 - q], P[5 - q], P[7 - q], P[9 - q]};
    }
  }
  __syncthreads();
  f4v acc[8];
#pragma unroll
  for (int i = 0; i < 8; ++i) acc[i] = f4v{0, 0, 0, 0};
  {
    const int g = lane >> 4, q4 = (lane >> 2) & 3, p4 = lane & 3, i = lane & 15;
    const char* kb = smem + K2_KC + c * 16640 + q4 * 4160 + 2 * (32 - 8 * g + 4 * p4) - 8 + 64 * (4 * hr);
    const char* ub = smem + K2_UL + c * 4608 + 2 * (240 + 16 * i + 8 * g) - 64 * (4 * hr);
#pragma unroll 1
    for (int it = 0; it < 4; ++it) {
      h8v uf[8], kf[8];
#pragma unroll
      for (int s = 0; s < 8; ++s) uf[s] = *(const h8v*)(ub + 512 * s);
#pragma unroll
      for (int q = 0; q < 8; ++q) {
        u2v lo = __builtin_bit_cast(u2v, trread(kb + 512 * q + 8)), hi = __builtin_bit_cast(u2v, trread(kb + 512 * q));
        kf[q] = __builtin_bit_cast(h8v, u4v{lo[0], lo[1], hi[0], hi[1]});
      }
#pragma unroll
      for (int q = 0; q < 8; ++q)
#pragma unroll
        for (int s = 0; s + q < 8; ++s) acc[s + q] = __builtin_amdgcn_mfma_f32_16x16x32_f16(kf[q], uf[s], acc[s + q], 0, 0, 0);
      kb += 64; ub -= 64;
    }
  }
  {
    f4v* xb = (f4v*)(smem + K2_XB);
#pragma unroll
    for (int j = 0; j < 4; ++j) xb[(w * 4 + j) * 64 + lane] = hr ? acc[j] : acc[4 + j];
    __syncthreads();
    const int g = lane >> 4, i = lane & 15;
#pragma unroll
    for (int j = 0; j < 4; ++j) {
      f4v y = (hr ? acc[4 + j] : acc[j]) + xb[((w ^ 4) * 4 + j) * 64 + lane];
      const int t = 256 * (4 * hr + j) + 16 * i + 4 * g;
      const u2v uu = *(const u2v*)(smem + K2_UL + c * 4608 + (256 + t) * 2);
      const float u0 = hlo(uu[0]), u1 = hhi(uu[0]), u2 = hlo(uu[1]), u3 = hhi(uu[1]);
      const unsigned y01 = pkbf(y[0] * Sc, y[1] * Sc), y23 = pkbf(y[2] * Sc, y[3] * Sc);
      const unsigned d01 = pkbf(u0 * Dk, u1 * Dk), d23 = pkbf(u2 * Dk, u3 * Dk);
      u2v so = {pkbf(bflo(y01) + bflo(d01), bfhi(y01) + bfhi(d01)), pkbf(bflo(y23) + bflo(d23), bfhi(y23) + bfhi(d23))};
      *(u2v*)(smem + K2_KC + c * 4096 + t * 2) = so;
    }
  }
  __syncthreads();
  {
    u2v s[4];
#pragma unroll
    for (int cc = 0; cc < 4; ++cc) s[cc] = *(const u2v*)(smem + K2_KC + cc * 4096 + tid * 8);
    unsigned o[8];
#pragma unroll
    for (int i = 0; i < 4; ++i) {
      float sv[4];
#pragma unroll
      for (int cc = 0; cc < 4; ++cc) sv[cc] = (i & 1) ? bfhi(s[cc][i >> 1]) : bflo(s[cc][i >> 1]);
      o[2 * i] = pkbf(bflo(xv[i][0]) + sv[0], bfhi(xv[i][0]) + sv[1]);
      o[2 * i + 1] = pkbf(bflo(xv[i][1]) + sv[2], bfhi(xv[i][1]) + sv[3]);
    }
    u4v* ho = (u4v*)(ws + WS_H + ((size_t)cg * SL + 4 * tid) * 8);
    ho[0] = u4v{o[0], o[1], o[2], o[3]};
    ho[1] = u4v{o[4], o[5], o[6], o[7]};
  }
}

constexpr int K3_WG = 16384, K3_WU = 16384 + 12288 + 32, K3_STAGE = 41216;
constexpr int K3_ST = 3 * K3_STAGE;
constexpr int K3_CS = K3_ST + 1024;
constexpr int K3_LDS = K3_CS + 768;

DEV void glds16(const void* g, void* l) {
  __builtin_amdgcn_global_load_lds((const __attribute__((address_space(1))) void*)g, (LDS_AS void*)l, 16, 0, 0);
}
DEV u2v dsr64(unsigned addr, int off) { return *(const LDS_AS u2v*)(size_t)(addr + off); }
DEV u2v dstr64(unsigned addr, int off) { return __builtin_bit_cast(u2v, __builtin_amdgcn_ds_read_tr16_b64_v4i16((LDS_AS s4v*)(size_t)(addr + off))); }
DEV unsigned opaque(unsigned a) { asm volatile("" : "+v"(a)); return a; }

struct K3Frag { u2v t[2][2]; u2v wv[3][2]; };

__global__ __launch_bounds__(512) void k3_gemm1(const u16* __restrict__ Wg, const u16* __restrict__ Wu, char* __restrict__ ws) {
  extern __shared__ __attribute__((aligned(16))) char smem[];
  const int tid = threadIdx.x, w = __builtin_amdgcn_readfirstlane(tid >> 6), lane = tid & 63;
  const int xcd = blockIdx.x & 7, jj = blockIdx.x >> 3;
  const int rb = 2 * xcd + (jj & 1), cb = jj >> 1;
  const int tok0 = 128 * rb, f0 = 96 * cb;
  if (w >= 4) {
    const int lw = w - 4;
    const char* hsrc = ws + WS_H + ((size_t)(4 * lw) * SL + tok0) * 8 + lane * 16;
    const char* wsrc = (const char*)((lw >> 1) ? Wu : Wg) + f0 * 2;
    unsigned woff[6];
#pragma unroll
    for (int i = 0; i < 6; ++i) { const int ch = 384 * (lw & 1) + 64 * i + lane; const int r = ch / 12; woff[i] = r * (FF * 2) + (ch - 12 * r) * 16; }
    const int wdst = ((lw >> 1) ? K3_WU : K3_WG) + 384 * (lw & 1) * 16;
    auto issue = [&](int t, int slot) {
      char* st = smem + slot * K3_STAGE;
#pragma unroll
      for (int i = 0; i < 4; ++i) glds16(hsrc + ((size_t)(16 * t + i) * SL) * 8, st + (4 * lw + i) * 1024);
#pragma unroll
      for (int i = 0; i < 6; ++i) glds16(wsrc + (size_t)t * (64 * FF * 2) + woff[i], st + wdst + i * 1024);
    };
    issue(0, 0); issue(1, 1); issue(2, 2);
    asm volatile("s_waitcnt vmcnt(20)" ::: "memory");
    __builtin_amdgcn_s_barrier();
    int slot = 0;
#pragma unroll 1
    for (int t = 0; t < 9; ++t) {
      if (t < 8) asm volatile("s_waitcnt vmcnt(10)" ::: "memory");
      else asm volatile("s_waitcnt vmcnt(0)" ::: "memory");
      __builtin_amdgcn_s_barrier();
      if (t < 7) issue(t + 3, slot);
      slot = slot == 2 ? 0 : slot + 1;
    }
    __builtin_amdgcn_s_barrier();
    return;
  }
  const int wr = w & 1, wc = w >> 1;
  const unsigned sbase = (unsigned)(size_t)(LDS_AS char*)smem;
  const int hh = lane >> 5;
  const unsigned ta = sbase + (2 * hh) * 1024 + (64 * wr + (lane & 31)) * 8;
  const unsigned wa = sbase + (((lane >> 4) & 1) ? K3_WU : K3_WG) + (8 * hh + ((lane >> 2) & 3)) * 192 + (48 * wc + 4 * (lane & 3)) * 2;
  f16v acc[3][2];
#pragma unroll
  for (int a = 0; a < 3; ++a)
#pragma unroll
    for (int b = 0; b < 2; ++b)
#pragma unroll
      for (int r = 0; r < 16; ++r) acc[a][b][r] = 0.f;
  float s1 = 0.f, s2 = 0.f;
  const b2v ones = __builtin_bit_cast(b2v, 0x3f803f80u);

#define K3_READ(F, KS)                                                                          \
  do {                                                                                          \
    F.t[0][0] = dsr64(t00, (KS) * 4096);       F.t[0][1] = dsr64(t01, (KS) * 4096);             \
    F.wv[0][0] = dstr64(wcur, (KS) * 3072);      F.wv[0][1] = dstr64(wcur, (KS) * 3072 + 768);  \
    F.t[1][0] = dsr64(t10, (KS) * 4096);       F.t[1][1] = dsr64(t11, (KS) * 4096);             \
    F.wv[1][0] = dstr64(wcur, (KS) * 3072 + 32); F.wv[1][1] = dstr64(wcur, (KS) * 3072 + 800);  \
    F.wv[2][0] = dstr64(wcur, (KS) * 3072 + 64); F.wv[2][1] = dstr64(wcur, (KS) * 3072 + 832);  \
  } while (0)
#define K3_MMA(F)                                                                                          \
  do {                                                                                                     \
    b8v t0 = __builtin_bit_cast(b8v, u4v{F.t[0][0][0], F.t[0][0][1], F.t[0][1][0], F.t[0][1][1]});         \
    b8v t1 = __builtin_bit_cast(b8v, u4v{F.t[1][0][0], F.t[1][0][1], F.t[1][1][0], F.t[1][1][1]});         \
    _Pragma("unroll") for (int a = 0; a < 3; ++a) {                                                        \
      b8v wv = __builtin_bit_cast(b8v, u4v{F.wv[a][0][0], F.wv[a][0][1], F.wv[a][1][0], F.wv[a][1][1]});   \
      acc[a][0] = __builtin_amdgcn_mfma_f32_32x32x16_bf16(wv, t0, acc[a][0], 0, 0, 0);                     \
      acc[a][1] = __builtin_amdgcn_mfma_f32_32x32x16_bf16(wv, t1, acc[a][1], 0, 0, 0);                     \
    }                                                                                                      \
    _Pragma("unroll") for (int e = 0; e < 4; ++e) {                                                        \
      const unsigned tu = wc ? F.t[1][e >> 1][e & 1] : F.t[0][e >> 1][e & 1];                              \
      const b2v v = __builtin_bit_cast(b2v, tu);                                                           \
      s1 = __builtin_amdgcn_fdot2_f32_bf16(v, ones, s1, false);                                            \
      s2 = __builtin_amdgcn_fdot2_f32_bf16(v, v, s2, false);                                               \
    }                                                                                                      \
  } while (0)
#define SB() __builtin_amdgcn_sched_barrier(0)

  K3Frag fa, fb;
  if (w == 0 && lane < 48) {
    const int m = lane / 24, j4 = lane - 24 * m;
    *(f4v*)(smem + K3_CS + m * 384 + j4 * 16) = *(const f4v*)(ws + WS_CS + (size_t)(m * FF + f0 + 4 * j4) * 4);
  }
  asm volatile("s_waitcnt lgkmcnt(0)" ::: "memory");
  __builtin_amdgcn_s_barrier();
  CFENCE();
  unsigned so = 0;
  unsigned t00 = opaque(ta), t01 = opaque(ta + 1024), t10 = opaque(ta + 256), t11 = opaque(ta + 1280), wcur = wa;
  K3_READ(fa, 0);
#pragma unroll 1
  for (int t = 0; t < 10; ++t) {
    CFENCE(); SB();
    K3_READ(fb, 1);
    K3_MMA(fa);
    CFENCE(); SB();
    K3_READ(fa, 2);
    K3_MMA(fb);
    CFENCE(); SB();
    K3_READ(fb, 3);
    K3_MMA(fa);
    SB();
    so = (so == 2 * K3_STAGE) ? 0 : so + K3_STAGE;
    t00 = opaque(ta + so); t01 = opaque(ta + 1024 + so); t10 = opaque(ta + 256 + so); t11 = opaque(ta + 1280 + so); wcur = wa + so;
    asm volatile("s_waitcnt lgkmcnt(0)" ::: "memory");
    if (t < 9) __builtin_amdgcn_s_barrier();
    CFENCE(); SB();
    K3_READ(fa, 0);
    K3_MMA(fb);
  }
  s1 += __shfl_xor(s1, 32); s2 += __shfl_xor(s2, 32);
  {
    const float mean = s1 * (1.f / DM);
    const float var = fmaxf(s2 * (1.f / DM) - mean * mean, 0.f);
    const float rstd = 1.f / sqrtf(var + 1e-5f);
    if (lane < 32) *(f2v*)(smem + K3_ST + (64 * wr + 32 * wc + lane) * 8) = f2v{rstd, -mean * rstd};
  }
  asm volatile("s_waitcnt lgkmcnt(0)" ::: "memory");
  __builtin_amdgcn_s_barrier();
  CFENCE();
#pragma unroll
  for (int b = 0; b < 2; ++b) {
    const f2v st = *(const f2v*)(smem + K3_ST + (64 * wr + 32 * b + (lane & 31)) * 8);
    const int tok = tok0 + 64 * wr + 32 * b + (lane & 31);
#pragma unroll
    for (int a = 0; a < 3; ++a) {
      const float* cs = (const float*)(smem + K3_CS) + 48 * wc + 16 * a + 4 * hh;
      const f4v cg0 = *(const f4v*)(cs), cg1 = *(const f4v*)(cs + 8), cu0 = *(const f4v*)(cs + 96), cu1 = *(const f4v*)(cs + 104);
      float hv[8];
#pragma unroll
      for (int r = 0; r < 8; ++r) {
        const float cgv = r < 4 ? cg0[r & 3] : cg1[r & 3], cuv = r < 4 ? cu0[r & 3] : cu1[r & 3];
        const float g = bfr(st[0] * acc[a][b][r] + st[1] * cgv);
        const float u = bfr(st[0] * acc[a][b][8 + r] + st[1] * cuv);
        const float e = __builtin_amdgcn_exp2f(g * (-2.302208198f - 0.1029432397f * g * g));
        const float ge = bfr(g * __builtin_amdgcn_rcpf(1.f + e));
        hv[r] = ge * u;
      }
      u4v o = {pkbf(hv[0], hv[1]), pkbf(hv[2], hv[3]), pkbf(hv[4], hv[5]), pkbf(hv[6], hv[7])};
      const int ftg = 6 * cb + 3 * wc + a;
      const int ks = ftg >> 1, g4 = 2 * (ftg & 1) + hh;
      *(u4v*)(ws + WS_HID + ((size_t)(((tok >> 6) * 48 + ks) * 4 + ((tok >> 4) & 3)) * 64 + 16 * g4 + (tok & 15)) * 16) = o;
    }
  }
}

constexpr int K4_LDS = 4 * 20 * 1024;
constexpr int K4_DEPTH = 4;
__global__ __launch_bounds__(256) void k4_gemm2(const char* __restrict__ ws, u16* __restrict__ out) {
  __shared__ __attribute__((aligned(16))) char smem[K4_LDS];
  const int tid = threadIdx.x, w = __builtin_amdgcn_readfirstlane(tid >> 6), lane = tid & 63;
  const int xcd = blockIdx.x & 7, jj = blockIdx.x >> 3;
  const int rb = 4 * xcd + (jj & 3), cb = jj >> 2;
  const u4v* hp = (const u4v*)(ws + WS_HID) + (size_t)(rb * 48 + 12 * w) * 256 + lane;
  const u4v* wp = (const u4v*)(ws + WS_W2T) + (size_t)(cb * 48 + 12 * w) * 320 + lane;
  u4v hf[K4_DEPTH][4], wf[K4_DEPTH][5];
#pragma unroll
  for (int s = 0; s < K4_DEPTH; ++s) {
#pragma unroll
    for (int i = 0; i < 4; ++i) hf[s][i] = hp[(s * 4 + i) * 64];
#pragma unroll
    for (int i = 0; i < 5; ++i) wf[s][i] = wp[(s * 5 + i) * 64];
  }
  const int g = lane >> 4, tok = 64 * rb + 16 * w + (lane & 15);
  u2v res[5];
#pragma unroll
  for (int i = 0; i < 5; ++i) res[i] = *(const u2v*)(ws + WS_H + ((size_t)(20 * cb + 4 * i + g) * SL + tok) * 8);
  f4v acc[4][5];
#pragma unroll
  for (int a = 0; a < 4; ++a)
#pragma unroll
    for (int b = 0; b < 5; ++b) acc[a][b] = f4v{0, 0, 0, 0};
#pragma unroll
  for (int s = 0; s < 12; ++s) {
    const int sl = s % K4_DEPTH;
#pragma unroll
    for (int a = 0; a < 4; ++a)
#pragma unroll
      for (int b = 0; b < 5; ++b)
        acc[a][b] = __builtin_amdgcn_mfma_f32_16x16x32_bf16(__builtin_bit_cast(b8v, wf[sl][b]), __builtin_bit_cast(b8v, hf[sl][a]), acc[a][b], 0, 0, 0);
    if (s + K4_DEPTH < 12) {
#pragma unroll
      for (int i = 0; i < 4; ++i) hf[sl][i] = hp[((s + K4_DEPTH) * 4 + i) * 64];
#pragma unroll
      for (int i = 0; i < 5; ++i) wf[sl][i] = wp[((s + K4_DEPTH) * 5 + i) * 64];
    }
  }
  f4v* red = (f4v*)smem;
#pragma unroll
  for (int a = 0; a < 4; ++a)
#pragma unroll
    for (int b = 0; b < 5; ++b) red[((w * 4 + a) * 5 + b) * 64 + lane] = acc[a][b];
  __syncthreads();
#pragma unroll
  for (int b = 0; b < 5; ++b) {
    f4v v = red[((0 * 4 + w) * 5 + b) * 64 + lane] + red[((1 * 4 + w) * 5 + b) * 64 + lane] + red[((2 * 4 + w) * 5 + b) * 64 + lane] +
            red[((3 * 4 + w) * 5 + b) * 64 + lane];
    const unsigned o01 = pkbf(v[0], v[1]), o23 = pkbf(v[2], v[3]);
    u2v o = {pkbf(bflo(res[b][0]) + bflo(o01), bfhi(res[b][0]) + bfhi(o01)), pkbf(bflo(res[b][1]) + bflo(o23), bfhi(res[b][1]) + bfhi(o23))};
    *(u2v*)(out + (size_t)tok * DM + 80 * cb + 16 * b + 4 * g) = o;
  }
}

extern "C" void kernel_launch(void* const* d_in, const int* in_sizes, int n_in, void* d_out, int out_size, void* d_ws, size_t ws_size,
                              hipStream_t stream) {
  const u16* x = (const u16*)d_in[0];
  const u16* logA = (const u16*)d_in[1];
  const u16* Aim = (const u16*)d_in[2];
  const u16* Cre = (const u16*)d_in[3];
  const u16* Cim = (const u16*)d_in[4];
  const u16* logdt = (const u16*)d_in[5];
  const u16* Dsk = (const u16*)d_in[6];
  const u16* Wg = (const u16*)d_in[8];
  const u16* Wu = (const u16*)d_in[9];
  const u16* Wd = (const u16*)d_in[10];
  char* ws = (char*)d_ws;
  (void)hipFuncSetAttribute((const void*)k2_conv, hipFuncAttributeMaxDynamicSharedMemorySize, K2_LDS);
  (void)hipFuncSetAttribute((const void*)k3_gemm1, hipFuncAttributeMaxDynamicSharedMemorySize, K3_LDS);
  k1_ln_kgen<<<448, 384, 0, stream>>>(x, logA, Aim, Cre, Cim, logdt, ws);
  k2_conv<<<256, 512, K2_LDS, stream>>>(x, Dsk, Wg, Wu, Wd, ws);
  k3_gemm1<<<256, 512, K3_LDS, stream>>>(Wg, Wu, ws);
  k4_gemm2<<<256, 256, 0, stream>>>(ws, (u16*)d_out);
}
```

```cpp
#include <hip/hip_runtime.h>
#include <hip/hip_bf16.h>
#include <stdint.h>

typedef _Float16 h2v __attribute__((ext_vector_type(2)));
typedef _Float16 h8v __attribute__((ext_vector_type(8)));
typedef __bf16 b2v __attribute__((ext_vector_type(2)));
typedef float f2v __attribute__((ext_vector_type(2)));
typedef float f4v __attribute__((ext_vector_type(4)));
typedef unsigned u2v __attribute__((ext_vector_type(2)));
typedef unsigned u4v __attribute__((ext_vector_type(4)));
typedef short s4v __attribute__((ext_vector_type(4)));
typedef unsigned short u16;
#define LDS_AS __attribute__((address_space(3)))
#define GLB_AS __attribute__((address_space(1)))
#define DEV __device__ __forceinline__

#define AUX_UT 16
#define AUX_H 16
#define AUX_W2T 16
#define AUX_HID 16
constexpr int CODE_PF = 33280;
constexpr int LN_PRIO = 3;
constexpr int K2_HPRIO = 1;
constexpr int SL = 2048;
constexpr int DM = 640;
constexpr int FF = 1536;
constexpr int KDS = 656;

constexpr size_t WS_UT = 0;
constexpr size_t WS_XT = WS_UT + (size_t)SL * DM * 2;
constexpr size_t WS_H = WS_XT + (size_t)SL * DM * 2;
constexpr size_t WS_HID = WS_H + (size_t)SL * DM * 2;
constexpr size_t WS_W2T = WS_HID + (size_t)SL * FF * 2;
constexpr int KDU = 1408;
constexpr size_t WS_KD = WS_W2T + (size_t)FF * DM * 2;
constexpr size_t WS_CS = WS_KD + (size_t)3 * DM * KDU;
constexpr size_t WS_ST = WS_CS + (size_t)2 * FF * 4;
constexpr size_t WS_CNT = WS_ST + (size_t)SL * 8;

DEV float bf2f(unsigned v) { return __builtin_bit_cast(float, v << 16); }
DEV float bflo(unsigned v) { return __builtin_bit_cast(float, v << 16); }
DEV float bfhi(unsigned v) { return __builtin_bit_cast(float, v & 0xffff0000u); }
DEV unsigned pkbf(float a, float b) { f2v v = {a, b}; return __builtin_bit_cast(unsigned, __builtin_convertvector(v, b2v)); }
DEV unsigned pkh(float a, float b) { f2v v = {a, b}; return __builtin_bit_cast(unsigned, __builtin_convertvector(v, h2v)); }
DEV float fexp(float a) { return __builtin_amdgcn_exp2f(a * 1.44269504088896340736f); }
DEV float bfr(float a) { return bflo(pkbf(a, a)); }
DEV float hlo(unsigned v) { return (float)__builtin_bit_cast(h2v, v)[0]; }
DEV float hhi(unsigned v) { return (float)__builtin_bit_cast(h2v, v)[1]; }
template <int CTRL> DEV float dppf(float v) {
  return __builtin_bit_cast(float, __builtin_amdgcn_update_dpp(0, __builtin_bit_cast(int, v), CTRL, 0xf, 0xf, false));
}
DEV float row16_sum(float v) { v += dppf<0x128>(v); v += dppf<0x124>(v); v += dppf<0x122>(v); v += dppf<0x121>(v); return v; }
DEV float rows_sum(float v) {
  const unsigned u = __builtin_bit_cast(unsigned, v);
  const auto a = __builtin_amdgcn_permlane16_swap(u, u, false, false);
  const float s = __builtin_bit_cast(float, (unsigned)a[0]) + __builtin_bit_cast(float, (unsigned)a[1]);
  const unsigned t = __builtin_bit_cast(unsigned, s);
  const auto b = __builtin_amdgcn_permlane32_swap(t, t, false, false);
  return __builtin_bit_cast(float, (unsigned)b[0]) + __builtin_bit_cast(float, (unsigned)b[1]);
}
DEV float rows_max(float v) {
  const unsigned u = __builtin_bit_cast(unsigned, v);
  const auto a = __builtin_amdgcn_permlane16_swap(u, u, false, false);
  const float s = fmaxf(__builtin_bit_cast(float, (unsigned)a[0]), __builtin_bit_cast(float, (unsigned)a[1]));
  const unsigned t = __builtin_bit_cast(unsigned, s);
  const auto b = __builtin_amdgcn_permlane32_swap(t, t, false, false);
  return fmaxf(__builtin_bit_cast(float, (unsigned)b[0]), __builtin_bit_cast(float, (unsigned)b[1]));
}
DEV float row16_max(float v) { v = fmaxf(v, dppf<0x128>(v)); v = fmaxf(v, dppf<0x124>(v)); v = fmaxf(v, dppf<0x122>(v)); v = fmaxf(v, dppf<0x121>(v)); return v; }
DEV s4v trread(const char* p) { return __builtin_amdgcn_ds_read_tr16_b64_v4i16((LDS_AS s4v*)p); }
#define CFENCE() asm volatile("" ::: "memory")
template <int AUX> DEV void st16x(void* base, unsigned off, u4v v) {
  __builtin_amdgcn_raw_buffer_store_b128(v, __builtin_amdgcn_make_buffer_rsrc(base, 0, 0x7fffffff, 0x00020000), off, 0, AUX);
}
#define st16(AUX, base, off, ...) st16x<AUX>(base, off, __VA_ARGS__)

typedef f2v cf;
#define CMUL_LO(D, A, B) "v_pk_mul_f32 " D ", " A ", " B " op_sel:[0,0] op_sel_hi:[0,1]\n\t"
#define CMUL_HI(D, A, B) "v_pk_fma_f32 " D ", " A ", " B ", " D " op_sel:[1,1,0] op_sel_hi:[1,0,1] neg_lo:[1,0,0]\n\t"
DEV cf cmul(cf a, cf b) {
  cf r;
  asm(CMUL_LO("%0", "%1", "%2") "s_nop 0\n\t" CMUL_HI("%0", "%1", "%2") : "=&v"(r) : "v"(a), "v"(b));
  return r;
}
DEV void cmul2(cf& r0, cf& r1, cf a0, cf b0, cf a1, cf b1) {
  asm(CMUL_LO("%0", "%2", "%3") CMUL_LO("%1", "%4", "%5") CMUL_HI("%0", "%2", "%3") CMUL_HI("%1", "%4", "%5")
      : "=&v"(r0), "=&v"(r1) : "v"(a0), "v"(b0), "v"(a1), "v"(b1));
}
DEV void cmul4(cf& r0, cf& r1, cf& r2, cf& r3, cf a0, cf a1, cf a2, cf a3, cf b) {
  asm(CMUL_LO("%0", "%4", "%8") CMUL_LO("%1", "%5", "%8") CMUL_LO("%2", "%6", "%8") CMUL_LO("%3", "%7", "%8")
      CMUL_HI("%0", "%4", "%8") CMUL_HI("%1", "%5", "%8") CMUL_HI("%2", "%6", "%8") CMUL_HI("%3", "%7", "%8")
      : "=&v"(r0), "=&v"(r1), "=&v"(r2), "=&v"(r3) : "v"(a0), "v"(a1), "v"(a2), "v"(a3), "v"(b));
}

__device__ const unsigned k2_tab[1892] __attribute__((aligned(16))) = {
  0x5100510, 0x510, 0x40002, 0x80006, 0xc000a, 0x20000e, 0x540, 0x0, 0xc000a, 0x10000e, 0x140012, 0x180016,
  0x1c001a, 0x30001e, 0x540, 0x0, 0x1c001a, 0x20001e, 0x240022, 0x280026, 0x2c002a, 0x40002e, 0x540, 0x0,
  0x2c002a, 0x30002e, 0x340032, 0x380036, 0x3c003a, 0x50003e, 0x540, 0x0, 0x3c003a, 0x40003e, 0x440042, 0x480046,
  0x4c004a, 0x60004e, 0x540, 0x0, 0x4c004a, 0x50004e, 0x540052, 0x580056, 0x5c005a, 0x70005e, 0x540, 0x0,
  0x5c005a, 0x60005e, 0x640062, 0x680066, 0x6c006a, 0x80006e, 0x540, 0x0, 0x6c006a, 0x70006e, 0x740072, 0x780076,
  0x7c007a, 0x90007e, 0x540, 0x0, 0x7c007a, 0x80007e, 0x840082, 0x880086, 0x8c008a, 0xa0008e, 0x540, 0x0,
  0x8c008a, 0x90008e, 0x940092, 0x980096, 0x9c009a, 0xb0009e, 0x540, 0x0, 0x9c009a, 0xa0009e, 0xa400a2, 0xa800a6,
  0xac00aa, 0xc000ae, 0x540, 0x0, 0xac00aa, 0xb000ae, 0xb400b2, 0xb800b6, 0xbc00ba, 0xd000be, 0x540, 0x0,
  0xbc00ba, 0xc000be, 0xc400c2, 0xc800c6, 0xcc00ca, 0xe000ce, 0x540, 0x0, 0xcc00ca, 0xd000ce, 0xd400d2, 0xd800d6,
  0xdc00da, 0xf000de, 0x540, 0x0, 0xdc00da, 0xe000de, 0xe400e2, 0xe800e6, 0xec00ea, 0x10000ee, 0x540, 0x0,
  0xec00ea, 0xf000ee, 0xf400f2, 0xf800f6, 0xfc00fa, 0x11000fe, 0x540, 0x0, 0xfc00fa, 0x10000fe, 0x1040102, 0x1080106,
  0x10c010a, 0x120010e, 0x540, 0x0, 0x10c010a, 0x110010e, 0x1140112, 0x1180116, 0x11c011a, 0x130011e, 0x540, 0x0,
  0x11c011a, 0x120011e, 0x1240122, 0x1280126, 0x12c012a, 0x140012e, 0x540, 0x0, 0x12c012a, 0x130012e, 0x1340132, 0x1380136,
  0x13c013a, 0x150013e, 0x540, 0x0, 0x13c013a, 0x140013e, 0x1440142, 0x1480146, 0x14c014a, 0x160014e, 0x540, 0x0,
  0x14c014a, 0x150014e, 0x1540152, 0x1580156, 0x15c015a, 0x170015e, 0x540, 0x0, 0x15c015a, 0x160015e, 0x1640162, 0x1680166,
  0x16c016a, 0x180016e, 0x540, 0x0, 0x16c016a, 0x170016e, 0x1740172, 0x1780176, 0x17c017a, 0x190017e, 0x540, 0x0,
  0x17c017a, 0x180017e, 0x1840182, 0x1880186, 0x18c018a, 0x1a0018e, 0x540, 0x0, 0x18c018a, 0x190018e, 0x1940192, 0x1980196,
  0x19c019a, 0x1b0019e, 0x540, 0x0, 0x19c019a, 0x1a0019e, 0x1a401a2, 0x1a801a6, 0x1ac01aa, 0x1c001ae, 0x540, 0x0,
  0x1ac01aa, 0x1b001ae, 0x1b401b2, 0x1b801b6, 0x1bc01ba, 0x1d001be, 0x540, 0x0, 0x1bc01ba, 0x1c001be, 0x1c401c2, 0x1c801c6,
  0x1cc01ca, 0x1e001ce, 0x540, 0x0, 0x1cc01ca, 0x1d001ce, 0x1d401d2, 0x1d801d6, 0x1dc01da, 0x1f001de, 0x540, 0x0,
  0x1dc01da, 0x1e001de, 0x1e401e2, 0x1e801e6, 0x1ec01ea, 0x20001ee, 0x540, 0x0, 0x1ec01ea, 0x1f001ee, 0x1f401f2, 0x1f801f6,
  0x1fc01fa, 0x21001fe, 0x540, 0x0, 0x1fc01fa, 0x20001fe, 0x2020200, 0x2040204, 0x2060204, 0x2200208, 0x540, 0x0,
  0x2060204, 0x2080208, 0x20a0208, 0x20c020c, 0x20e020c, 0x2300210, 0x540, 0x0, 0x20e020c, 0x2100210, 0x2120210, 0x2140214,
  0x2160214, 0x2400218, 0x540, 0x0, 0x2160214, 0x2180218, 0x21a0218, 0x21c021c, 0x21e021c, 0x2500220, 0x540, 0x0,
  0x21e021c, 0x2200220, 0x2220220, 0x2240224, 0x2260224, 0x2600228, 0x540, 0x0, 0x2260224, 0x2280228, 0x22a0228, 0x22c022c,
  0x22e022c, 0x2700230, 0x540, 0x0, 0x22e022c, 0x2300230, 0x2320230, 0x2340234, 0x2360234, 0x2800238, 0x540, 0x0,
  0x2360234, 0x2380238, 0x23a0238, 0x23c023c, 0x23e023c, 0x2900240, 0x540, 0x0, 0x23e023c, 0x2400240, 0x2420240, 0x2440244,
  0x2460244, 0x2a00248, 0x540, 0x0, 0x2460244, 0x2480248, 0x24a0248, 0x24c024c, 0x24e024c, 0x2b00250, 0x540, 0x0,
  0x24e024c, 0x2500250, 0x2520250, 0x2540254, 0x2560254, 0x2c00258, 0x540, 0x0, 0x2560254, 0x2580258, 0x25a0258, 0x25c025c,
  0x25e025c, 0x2d00260, 0x540, 0x0, 0x25e025c, 0x2600260, 0x2620260, 0x2640264, 0x2660264, 0x2e00268, 0x540, 0x0,
  0x2660264, 0x2680268, 0x26a0268, 0x26c026c, 0x26e026c, 0x2f00270, 0x540, 0x0, 0x26e026c, 0x2700270, 0x2720270, 0x2740274,
  0x2760274, 0x3000278, 0x540, 0x0, 0x2760274, 0x2780278, 0x27a0278, 0x27c027c, 0x27e027c, 0x3100280, 0x540, 0x0,
  0x27e027c, 0x2800280, 0x2820280, 0x2840284, 0x2860284, 0x3200288, 0x540, 0x0, 0x2860284, 0x2880288, 0x28a0288, 0x28c028c,
  0x28e028c, 0x3300290, 0x540, 0x0, 0x28e028c, 0x2900290, 0x2920290, 0x2940294, 0x2960294, 0x3400298, 0x540, 0x0,
  0x2960294, 0x2980298, 0x29a0298, 0x29c029c, 0x29e029c, 0x35002a0, 0x540, 0x0, 0x29e029c, 0x2a002a0, 0x2a202a0, 0x2a402a4,
  0x2a602a4, 0x36002a8, 0x540, 0x0, 0x2a602a4, 0x2a802a8, 0x2aa02a8, 0x2ac02ac, 0x2ae02ac, 0x37002b0, 0x540, 0x0,
  0x2ae02ac, 0x2b002b0, 0x2b202b0, 0x2b402b4, 0x2b602b4, 0x38002b8, 0x540, 0x0, 0x2b602b4, 0x2b802b8, 0x2ba02b8, 0x2bc02bc,
  0x2be02bc, 0x39002c0, 0x540, 0x0, 0x2be02bc, 0x2c002c0, 0x2c202c0, 0x2c402c4, 0x2c602c4, 0x3a002c8, 0x540, 0x0,
  0x2c602c4, 0x2c802c8, 0x2ca02c8, 0x2cc02cc, 0x2ce02cc, 0x3b002d0, 0x540, 0x0, 0x2ce02cc, 0x2d002d0, 0x2d202d0, 0x2d402d4,
  0x2d602d4, 0x3c002d8, 0x540, 0x0, 0x2d602d4, 0x2d802d8, 0x2da02d8, 0x2dc02dc, 0x2de02dc, 0x3d002e0, 0x540, 0x0,
  0x2de02dc, 0x2e002e0, 0x2e202e0, 0x2e402e4, 0x2e602e4, 0x3e002e8, 0x540, 0x0, 0x2e602e4, 0x2e802e8, 0x2ea02e8, 0x2ec02ec,
  0x2ee02ec, 0x3f002f0, 0x540, 0x0, 0x2ee02ec, 0x2f002f0, 0x2f202f0, 0x2f402f4, 0x2f602f4, 0x40002f8, 0x540, 0x0,
  0x2f602f4, 0x2f802f8, 0x2fa02f8, 0x2fc02fc, 0x2fe02fc, 0x4100300, 0x540, 0x0, 0x2fe02fc, 0x3000300, 0x3000300, 0x5100510,
  0x5100510, 0x4200510, 0x540, 0x0, 0x5100510, 0x5100510, 0x4000510, 0x4000400, 0x4000400, 0x4b00400, 0x540, 0x0,
  0x4000400, 0x4000400, 0x5100510, 0x5100510, 0x5100510, 0x4c00510, 0x540, 0x0, 0x5100510, 0x3080510, 0x3180310, 0x3280320,
  0x3380330, 0x15400340, 0xc0, 0x0, 0x3380330, 0x3480340, 0x3580350, 0x3680360, 0x3780370, 0x15500380, 0xc0, 0x0,
  0x3780370, 0x3880380, 0x3980390, 0x3a803a0, 0x3b803b0, 0x156003c0, 0xc0, 0x0, 0x3b803b0, 0x3c803c0, 0x3d803d0, 0x3e803e0,
  0x3f803f0, 0x15700510, 0xc0, 0x0, 0x3f803f0, 0x5100510, 0x5100510, 0x5100510, 0x5100510, 0x15800510, 0xc0, 0x0,
  0x5100510, 0x3020510, 0x312030a, 0x322031a, 0x332032a, 0x1840033a, 0xc0, 0x0, 0x332032a, 0x342033a, 0x352034a, 0x362035a,
  0x372036a, 0x1850037a, 0xc0, 0x0, 0x372036a, 0x382037a, 0x392038a, 0x3a2039a, 0x3b203aa, 0x186003ba, 0xc0, 0x0,
  0x3b203aa, 0x3c203ba, 0x3d203ca, 0x3e203da, 0x3f203ea, 0x187003fa, 0xc0, 0x0, 0x3f203ea, 0x51003fa, 0x5100510, 0x5100510,
  0x5100510, 0x18800510, 0xc0, 0x0, 0x5100510, 0x3040510, 0x314030c, 0x324031c, 0x334032c, 0x1b40033c, 0xc0, 0x0,
  0x334032c, 0x344033c, 0x354034c, 0x364035c, 0x374036c, 0x1b50037c, 0xc0, 0x0, 0x374036c, 0x384037c, 0x394038c, 0x3a4039c,
  0x3b403ac, 0x1b6003bc, 0xc0, 0x0, 0x3b403ac, 0x3c403bc, 0x3d403cc, 0x3e403dc, 0x3f403ec, 0x1b7003fc, 0xc0, 0x0,
  0x3f403ec, 0x51003fc, 0x5100510, 0x5100510, 0x5100510, 0x1b800510, 0xc0, 0x0, 0x5100510, 0x3060510, 0x316030e, 0x326031e,
  0x336032e, 0x1e40033e, 0xc0, 0x0, 0x336032e, 0x346033e, 0x356034e, 0x366035e, 0x376036e, 0x1e50037e, 0xc0, 0x0,
  0x376036e, 0x386037e, 0x396038e, 0x3a6039e, 0x3b603ae, 0x1e6003be, 0xc0, 0x0, 0x3b603ae, 0x3c603be, 0x3d603ce, 0x3e603de,
  0x3f603ee, 0x1e7003fe, 0xc0, 0x0, 0x3f603ee, 0x51003fe, 0x5100510, 0x5100510, 0x5100510, 0x1e800510, 0xc0, 0x0,
  0x5100510, 0x4040510, 0x40c0408, 0x4140410, 0x41c0418, 0x21400420, 0xc0, 0x0, 0x41c0418, 0x4240420, 0x42c0428, 0x4340430,
  0x43c0438, 0x21500440, 0xc0, 0x0, 0x43c0438, 0x4440440, 0x44c0448, 0x4540450, 0x45c0458, 0x21600460, 0xc0, 0x0,
  0x45c0458, 0x4640460, 0x46c0468, 0x4740470, 0x47c0478, 0x21700480, 0xc0, 0x0, 0x47c0478, 0x4840480, 0x48c0488, 0x4940490,
  0x49c0498, 0x218004a0, 0xc0, 0x0, 0x49c0498, 0x4a404a0, 0x4ac04a8, 0x4b404b0, 0x4bc04b8, 0x219004c0, 0xc0, 0x0,
  0x4bc04b8, 0x4c404c0, 0x4cc04c8, 0x4d404d0, 0x4dc04d8, 0x21a004e0, 0xc0, 0x0, 0x4dc04d8, 0x4e404e0, 0x4ec04e8, 0x4f404f0,
  0x4fc04f8, 0x21b00500, 0xc0, 0x0, 0x5100510, 0x4020510, 0x40a0406, 0x412040e, 0x41a0416, 0x2440041e, 0xc0, 0x0,
  0x41a0416, 0x422041e, 0x42a0426, 0x432042e, 0x43a0436, 0x2450043e, 0xc0, 0x0, 0x43a0436, 0x442043e, 0x44a0446, 0x452044e,
  0x45a0456, 0x2460045e, 0xc0, 0x0, 0x45a0456, 0x462045e, 0x46a0466, 0x472046e, 0x47a0476, 0x2470047e, 0xc0, 0x0,
  0x47a0476, 0x482047e, 0x48a0486, 0x492048e, 0x49a0496, 0x2480049e, 0xc0, 0x0, 0x49a0496, 0x4a2049e, 0x4aa04a6, 0x4b204ae,
  0x4ba04b6, 0x249004be, 0xc0, 0x0, 0x4ba04b6, 0x4c204be, 0x4ca04c6, 0x4d204ce, 0x4da04d6, 0x24a004de, 0xc0, 0x0,
  0x4da04d6, 0x4e204de, 0x4ea04e6, 0x4f204ee, 0x4fa04f6, 0x24b004fe, 0xc0, 0x0, 0x0, 0x0, 0x0, 0x3c000000,
  0x0, 0x0, 0x0, 0x0, 0x0, 0x0, 0x0, 0x0, 0x0, 0x0, 0x0, 0x0,
  0x0, 0x0, 0x0, 0x0, 0x0, 0x0, 0x0, 0x0, 0x0, 0x0, 0x0, 0x0,
  0x0, 0x0, 0x0, 0x0, 0x0, 0x0, 0x0, 0x0, 0x0, 0x0, 0x0, 0x0,
  0x0, 0x0, 0x0, 0x0, 0x0, 0x0, 0x0, 0x0, 0x0, 0x0, 0x0, 0x0,
  0x0, 0x0, 0x0, 0x0, 0x0, 0x0, 0x0, 0x0, 0x0, 0x0, 0x0, 0x0,
  0x3c003c00, 0x0, 0x0, 0x0, 0x3c003c00, 0x3c00, 0x0, 0x0, 0x3c000000, 0x3c003c00, 0x0, 0x0,
  0x0, 0x3c003c00, 0x3c00, 0x0, 0x0, 0x3c000000, 0x3c003c00, 0x0, 0x0, 0x0, 0x3c003c00, 0x3c00,
  0x0, 0x0, 0x3c000000, 0x3c003c00, 0x0, 0x0, 0x0, 0x3c003c00, 0x0, 0x0, 0x0, 0x3c000000,
  0x0, 0x0, 0x0, 0x0, 0x0, 0x0, 0x0, 0x0, 0x0, 0x0, 0x0, 0x0,
  0x0, 0x0, 0x0, 0x0, 0x0, 0x0, 0x0, 0x0, 0x0, 0x0, 0x0, 0x0,
  0x0, 0x0, 0x0, 0x0, 0x0, 0x0, 0x0, 0x0, 0x0, 0x0, 0x0, 0x0,
  0x0, 0x0, 0x0, 0x0, 0x0, 0x0, 0x0, 0x0, 0x0, 0x0, 0x0, 0x0,
  0x0, 0x0, 0x0, 0x0, 0x0, 0x0, 0x0, 0x0, 0x3c00, 0x0, 0x0, 0x0,
  0x3c003c00, 0x0, 0x0, 0x0, 0x3c003c00, 0x3c00, 0x0, 0x0, 0x3c000000, 0x3c003c00, 0x0, 0x0,
  0x0, 0x3c003c00, 0x3c00, 0x0, 0x0, 0x3c000000, 0x3c003c00, 0x0, 0x0, 0x0, 0x3c003c00, 0x3c00,
  0x0, 0x0, 0x3c000000, 0x3c003c00, 0x0, 0x0, 0x0, 0x3c003c00, 0x0, 0x0, 0x0, 0x0,
  0x0, 0x0, 0x0, 0x0, 0x0, 0x0, 0x0, 0x0, 0x0, 0x0, 0x0, 0x0,
  0x0, 0x0, 0x0, 0x0, 0x0, 0x0, 0x0, 0x0, 0x0, 0x0, 0x0, 0x0,
  0x0, 0x0, 0x0, 0x0, 0x0, 0x0, 0x0, 0x0, 0x0, 0x0, 0x0, 0x0,
  0x0, 0x0, 0x0, 0x0, 0x0, 0x0, 0x0, 0x0, 0x0, 0x0, 0x0, 0x0,
  0x0, 0x0, 0x0, 0x0, 0x0, 0x0, 0x0, 0x0, 0x3c00, 0x0, 0x0, 0x0,
  0x0, 0x0, 0x0, 0x3c003c00, 0x0, 0x0, 0x0, 0x3c000000, 0x0, 0x0, 0x0, 0x0,
  0x0, 0x0, 0x0, 0x0, 0x0, 0x0, 0x0, 0x0, 0x0, 0x0, 0x0, 0x0,
  0x0, 0x0, 0x0, 0x0, 0x0, 0x0, 0x0, 0x0, 0x0, 0x0, 0x0, 0x0,
  0x0, 0x0, 0x0, 0x0, 0x0, 0x0, 0x0, 0x0, 0x0, 0x0, 0x0, 0x0,
  0x0, 0x0, 0x0, 0x0, 0x0, 0x0, 0x0, 0x0, 0x0, 0x0, 0x0, 0x0,
  0x0, 0x0, 0x0, 0x0, 0x3c003c00, 0x3c00, 0x0, 0x0, 0x3c003c00, 0x3c003c00, 0x0, 0x0,
  0x3c003c00, 0x3c003c00, 0x3c00, 0x0, 0x3c000000, 0x3c003c00, 0x3c003c00, 0x0, 0x0, 0x3c003c00, 0x3c003c00, 0x3c00,
  0x0, 0x3c000000, 0x3c003c00, 0x3c003c00, 0x0, 0x0, 0x3c003c00, 0x3c003c00, 0x0, 0x0, 0x3c000000, 0x3c003c00,
  0x0, 0x0, 0x0, 0x3c003c00, 0x0, 0x0, 0x0, 0x3c000000, 0x0, 0x0, 0x0, 0x0,
  0x0, 0x0, 0x0, 0x0, 0x0, 0x0, 0x0, 0x0, 0x0, 0x0, 0x0, 0x0,
  0x0, 0x0, 0x0, 0x0, 0x0, 0x0, 0x0, 0x0, 0x0, 0x0, 0x0, 0x0,
  0x0, 0x0, 0x0, 0x0, 0x0, 0x0, 0x0, 0x0, 0x0, 0x0, 0x0, 0x0,
  0x0, 0x0, 0x0, 0x0, 0x0, 0x0, 0x0, 0x0, 0x3c00, 0x0, 0x0, 0x0,
  0x3c003c00, 0x0, 0x0, 0x0, 0x3c003c00, 0x3c00, 0x0, 0x0, 0x3c003c00, 0x3c003c00, 0x0, 0x0,
  0x3c003c00, 0x3c003c00, 0x3c00, 0x0, 0x3c000000, 0x3c003c00, 0x3c003c00, 0x0, 0x0, 0x3c003c00, 0x3c003c00, 0x3c00,
  0x0, 0x3c000000, 0x3c003c00, 0x3c003c00, 0x0, 0x0, 0x3c003c00, 0x3c003c00, 0x0, 0x0, 0x3c000000, 0x3c003c00,
  0x0, 0x0, 0x0, 0x0, 0x0, 0x0, 0x0, 0x0, 0x0, 0x0, 0x0, 0x0,
  0x0, 0x0, 0x0, 0x0, 0x0, 0x0, 0x0, 0x0, 0x0, 0x0, 0x0, 0x0,
  0x0, 0x0, 0x0, 0x0, 0x0, 0x0, 0x0, 0x0, 0x0, 0x0, 0x0, 0x0,
  0x0, 0x0, 0x0, 0x0, 0x0, 0x0, 0x0, 0x0, 0x0, 0x0, 0x0, 0x0,
  0x0, 0x0, 0x0, 0x0, 0x0, 0x0, 0x0, 0x0, 0x3c00, 0x0, 0x0, 0x0,
  0x3c003c00, 0x0, 0x0, 0x0, 0x0, 0x0, 0x3c000000, 0x3c003c00, 0x0, 0x0, 0x0, 0x3c003c00,
  0x0, 0x0, 0x0, 0x3c000000, 0x0, 0x0, 0x0, 0x0, 0x0, 0x0, 0x0, 0x0,
  0x0, 0x0, 0x0, 0x0, 0x0, 0x0, 0x0, 0x0, 0x0, 0x0, 0x0, 0x0,
  0x0, 0x0, 0x0, 0x0, 0x0, 0x0, 0x0, 0x0, 0x0, 0x0, 0x0, 0x0,
  0x0, 0x0, 0x0, 0x0, 0x0, 0x0, 0x0, 0x0, 0x0, 0x0, 0x0, 0x0,
  0x0, 0x0, 0x0, 0x0, 0x0, 0x0, 0x0, 0x0, 0x3c003c00, 0x3c003c00, 0x0, 0x0,
  0x3c003c00, 0x3c003c00, 0x3c00, 0x0, 0x3c003c00, 0x3c003c00, 0x3c003c00, 0x0, 0x3c003c00, 0x3c003c00, 0x3c003c00, 0x3c00,
  0x3c000000, 0x3c003c00, 0x3c003c00, 0x3c003c00, 0x0, 0x3c003c00, 0x3c003c00, 0x3c003c00, 0x0, 0x3c000000, 0x3c003c00, 0x3c003c00,
  0x0, 0x0, 0x3c003c00, 0x3c003c00, 0x0, 0x0, 0x3c000000, 0x3c003c00, 0x0, 0x0, 0x0, 0x3c003c00,
  0x0, 0x0, 0x0, 0x3c000000, 0x0, 0x0, 0x0, 0x0, 0x0, 0x0, 0x0, 0x0,
  0x0, 0x0, 0x0, 0x0, 0x0, 0x0, 0x0, 0x0, 0x0, 0x0, 0x0, 0x0,
  0x0, 0x0, 0x0, 0x0, 0x0, 0x0, 0x0, 0x0, 0x0, 0x0, 0x0, 0x0,
  0x0, 0x0, 0x0, 0x0, 0x0, 0x0, 0x0, 0x0, 0x3c00, 0x0, 0x0, 0x0,
  0x3c003c00, 0x0, 0x0, 0x0, 0x3c003c00, 0x3c00, 0x0, 0x0, 0x3c003c00, 0x3c003c00, 0x0, 0x0,
  0x3c003c00, 0x3c003c00, 0x3c00, 0x0, 0x3c003c00, 0x3c003c00, 0x3c003c00, 0x0, 0x3c003c00, 0x3c003c00, 0x3c003c00, 0x3c00,
  0x3c000000, 0x3c003c00, 0x3c003c00, 0x3c003c00, 0x0, 0x3c003c00, 0x3c003c00, 0x3c003c00, 0x0, 0x3c000000, 0x3c003c00, 0x3c003c00,
  0x0, 0x0, 0x3c003c00, 0x3c003c00, 0x0, 0x0, 0x0, 0x0, 0x0, 0x0, 0x0, 0x0,
  0x0, 0x0, 0x0, 0x0, 0x0, 0x0, 0x0, 0x0, 0x0, 0x0, 0x0, 0x0,
  0x0, 0x0, 0x0, 0x0, 0x0, 0x0, 0x0, 0x0, 0x0, 0x0, 0x0, 0x0,
  0x0, 0x0, 0x0, 0x0, 0x0, 0x0, 0x0, 0x0, 0x0, 0x0, 0x0, 0x0,
  0x0, 0x0, 0x0, 0x0, 0x0, 0x0, 0x0, 0x0, 0x3c00, 0x0, 0x0, 0x0,
  0x3c003c00, 0x0, 0x0, 0x0, 0x3c003c00, 0x3c00, 0x0, 0x0, 0x0, 0x0, 0x3c003c00, 0x3c003c00,
  0x0, 0x0, 0x3c000000, 0x3c003c00, 0x0, 0x0, 0x0, 0x3c003c00, 0x0, 0x0, 0x0, 0x3c000000,
  0x0, 0x0, 0x0, 0x0, 0x0, 0x0, 0x0, 0x0, 0x0, 0x0, 0x0, 0x0,
  0x0, 0x0, 0x0, 0x0, 0x0, 0x0, 0x0, 0x0, 0x0, 0x0, 0x0, 0x0,
  0x0, 0x0, 0x0, 0x0, 0x0, 0x0, 0x0, 0x0, 0x0, 0x0, 0x0, 0x0,
  0x0, 0x0, 0x0, 0x0, 0x0, 0x0, 0x0, 0x0, 0x0, 0x0, 0x0, 0x0,
  0x3c003c00, 0x3c003c00, 0x3c00, 0x0, 0x3c003c00, 0x3c003c00, 0x3c003c00, 0x0, 0x3c003c00, 0x3c003c00, 0x3c003c00, 0x3c00,
  0x3c003c00, 0x3c003c00, 0x3c003c00, 0x3c003c00, 0x3c003c00, 0x3c003c00, 0x3c003c00, 0x3c003c00, 0x3c000000, 0x3c003c00, 0x3c003c00, 0x3c003c00,
  0x0, 0x3c003c00, 0x3c003c00, 0x3c003c00, 0x0, 0x3c000000, 0x3c003c00, 0x3c003c00, 0x0, 0x0, 0x3c003c00, 0x3c003c00,
  0x0, 0x0, 0x3c000000, 0x3c003c00, 0x0, 0x0, 0x0, 0x3c003c00, 0x0, 0x0, 0x0, 0x3c000000,
  0x0, 0x0, 0x0, 0x0, 0x0, 0x0, 0x0, 0x0, 0x0, 0x0, 0x0, 0x0,
  0x0, 0x0, 0x0, 0x0, 0x0, 0x0, 0x0, 0x0, 0x0, 0x0, 0x0, 0x0,
  0x0, 0x0, 0x0, 0x0, 0x0, 0x0, 0x0, 0x0, 0x3c00, 0x0, 0x0, 0x0,
  0x3c003c00, 0x0, 0x0, 0x0, 0x3c003c00, 0x3c00, 0x0, 0x0, 0x3c003c00, 0x3c003c00, 0x0, 0x0,
  0x3c003c00, 0x3c003c00, 0x3c00, 0x0, 0x3c003c00, 0x3c003c00, 0x3c003c00, 0x0, 0x3c003c00, 0x3c003c00, 0x3c003c00, 0x3c00,
  0x3c003c00, 0x3c003c00, 0x3c003c00, 0x3c003c00, 0x3c003c00, 0x3c003c00, 0x3c003c00, 0x3c003c00, 0x3c000000, 0x3c003c00, 0x3c003c00, 0x3c003c00,
  0x0, 0x3c003c00, 0x3c003c00, 0x3c003c00, 0x0, 0x3c000000, 0x3c003c00, 0x3c003c00, 0x0, 0x0, 0x0, 0x0,
  0x0, 0x0, 0x0, 0x0, 0x0, 0x0, 0x0, 0x0, 0x0, 0x0, 0x0, 0x0,
  0x0, 0x0, 0x0, 0x0, 0x0, 0x0, 0x0, 0x0, 0x0, 0x0, 0x0, 0x0,
  0x0, 0x0, 0x0, 0x0, 0x0, 0x0, 0x0, 0x0, 0x0, 0x0, 0x0, 0x0,
  0x0, 0x0, 0x0, 0x0, 0x0, 0x0, 0x0, 0x0, 0x3c00, 0x0, 0x0, 0x0,
  0x3c003c00, 0x0, 0x0, 0x0, 0x3c003c00, 0x3c00, 0x0, 0x0, 0x3c003c00, 0x3c003c00, 0x0, 0x0,
  0x5400000, 0x5400010, 0x5400430, 0x5400440, 0x5400450, 0x5400460, 0x5400470, 0x5400480, 0x5400490, 0x54004a0, 0x54004d0, 0x54004e0,
  0x54004f0, 0xc01500, 0xc01510, 0xc01520, 0xc01530, 0xc01590, 0xc01800, 0xc01810, 0xc01820, 0xc01830, 0xc01890, 0xc01b00,
  0xc01b10, 0xc01b20, 0xc01b30, 0xc01b90, 0xc01e00, 0xc01e10, 0xc01e20, 0xc01e30, 0xc01e90, 0xc02100, 0xc02110, 0xc02120,
  0xc02130, 0xc02400, 0xc02410, 0xc02420, 0xc02430, 0x0, 0x0, 0x0,
};
constexpr int K2T_BAND = 103 * 8, K2T_ZERO = K2T_BAND + 4 * 64 * 4;

constexpr int K1_REG = 16 * 272;
constexpr int K1_LDS = 16 * 1312;
constexpr int K1_NLN = 256, K1_NKG = 960;

__global__ __launch_bounds__(128) void k1_ln_kgen(const u16* __restrict__ x, const u16* __restrict__ logA, const u16* __restrict__ Aim,
                                                  const u16* __restrict__ Cre, const u16* __restrict__ Cim, const u16* __restrict__ logdt,
                                                  const char* __restrict__ Wg, const char* __restrict__ Wu, const char* __restrict__ Wd,
                                                  char* __restrict__ ws) {
  __shared__ __attribute__((aligned(16))) char smem[K1_LDS];
  const int tid = threadIdx.x, w = __builtin_amdgcn_readfirstlane(tid >> 6), lane = tid & 63;
  unsigned long long pc0;
  asm volatile("s_getpc_b64 %0" : "=s"(pc0));
  if (blockIdx.x >= K1_NLN + K1_NKG) {
    const int pw = ((blockIdx.x - (K1_NLN + K1_NKG)) >> 3) * 2 + w;
    unsigned d[2];
    {
      constexpr int PFS = 32768, NP = DM * FF * 2 / PFS;
      const int n = min(pw * 64 + lane, 3 * NP - 1), m = n / NP, r = n - m * NP;
      d[0] = *(const unsigned*)((m == 0 ? Wg : m == 1 ? Wu : Wd) + (size_t)r * PFS);
    }
    const char* cp = (lane & 15) < 9 ? (const char*)(pc0 & ~255ull) + min((pw * 9 + (lane & 15)) * 128, CODE_PF)
                                     : (const char*)k2_tab + min((pw * 2 + (lane & 1)) * 128, (int)sizeof(k2_tab) - 4);
    d[1] = *(const unsigned*)cp;
    asm volatile("" ::"v"(d[0]), "v"(d[1]));
    return;
  }
  if (blockIdx.x < K1_NLN) {
    const int tb = blockIdx.x;
    __builtin_amdgcn_s_setprio(LN_PRIO);
    if (tid < 8 && !(tb & 1)) *(u4v*)(ws + WS_ST + tb * 64 + tid * 16) = u4v{0, 0, 0, 0};
    if (tid >= 8 && tid < 16 && tb < 64) *(u4v*)(ws + WS_CNT + tb * 128 + (tid - 8) * 16) = u4v{0, 0, 0, 0};
    const int row = 4 * w + (lane >> 4), c = lane & 15;
    const u4v* xr = (const u4v*)(x + (size_t)(8 * tb + row) * DM);
    u4v v[5];
#pragma unroll
    for (int i = 0; i < 5; ++i) v[i] = xr[c + 16 * i];
    f2v s2 = {0.f, 0.f};
#pragma unroll
    for (int i = 0; i < 5; ++i)
#pragma unroll
      for (int j = 0; j < 4; ++j) s2 += f2v{bflo(v[i][j]), bfhi(v[i][j])};
    const float mean = row16_sum(s2[0] + s2[1]) * (1.f / DM);
    f2v q2 = {0.f, 0.f};
#pragma unroll
    for (int i = 0; i < 5; ++i)
#pragma unroll
      for (int j = 0; j < 4; ++j) { const f2v dl = f2v{bflo(v[i][j]), bfhi(v[i][j])} - mean; q2 += dl * dl; }
    const float q = row16_sum(q2[0] + q2[1]);
    const float mb = bfr(mean), vb = bfr(q * (1.f / DM));
    const float den = bfr(sqrtf(bfr(vb + 1e-5f)));
    const float rden = 1.f / den;
#pragma unroll
    for (int i = 0; i < 5; ++i) {
      u4v o;
#pragma unroll
      for (int j = 0; j < 4; ++j) {
        const f2v t0 = f2v{bflo(v[i][j]), bfhi(v[i][j])} - mb;
        const unsigned t1 = pkbf(t0[0], t0[1]);
        const f2v t2 = f2v{bflo(t1), bfhi(t1)} * rden;
        const unsigned t3 = pkbf(t2[0], t2[1]);
        o[j] = pkh(bflo(t3), bfhi(t3));
      }
      *(u4v*)(smem + row * 1312 + (c + 16 * i) * 16) = o;
      *(u4v*)(smem + (8 + row) * 1312 + (c + 16 * i) * 16) = v[i];
    }
    __syncthreads();
    {
      const int G = lane >> 4, q = (lane & 15) >> 2, p = lane & 3, i = lane & 15;
#pragma unroll
      for (int it = 0; it < 5; ++it) {
        const int cb = 4 * (w * 5 + it) + G;
        const char* a = smem + q * 1312 + (16 * cb + 4 * p) * 2;
        u2v a0 = __builtin_bit_cast(u2v, trread(a)), a1 = __builtin_bit_cast(u2v, trread(a + 4 * 1312));
        u2v b0 = __builtin_bit_cast(u2v, trread(a + 8 * 1312)), b1 = __builtin_bit_cast(u2v, trread(a + 12 * 1312));
        const int ch = 16 * cb + i;
        const unsigned o = (unsigned)((((ch >> 3) * 256 + tb) * 64 + (ch & 7) * 8) * 2);
        st16(AUX_UT, ws + WS_UT, o, u4v{a0[0], a0[1], a1[0], a1[1]});
        st16(AUX_UT, ws + WS_XT, o, u4v{b0[0], b0[1], b1[0], b1[1]});
      }
    }
    return;
  }
  const int unit = 2 * (blockIdx.x - K1_NLN) + w, d = unit / 3, n = unit * 64 + lane;
  const float dt = bfr(fexp(bf2f(logdt[d])));
  const float are = -bfr(fexp(bf2f(logA[n])));
  const float zr = are * dt, zi = bf2f(Aim[n]) * dt;
  const float cr = bf2f(Cre[n]) * dt, ci = bf2f(Cim[n]) * dt;
  float cm = row16_max(fmaxf(fabsf(cr), fabsf(ci)));
  cm = rows_max(cm);
  int ex = (__builtin_bit_cast(unsigned, cm) >> 23) & 0xff;
  ex = min(max(ex, 1), 240);
  const float gs = __builtin_bit_cast(float, (unsigned)(253 - ex) << 23);
  const float zr2 = zr * 1.44269504088896340736f, zi2 = zi * 0.15915494309189533577f;
  auto E = [&](float p) { float m = __builtin_amdgcn_exp2f(zr2 * p); float ph = __builtin_amdgcn_fractf(zi2 * p);
                          return cf{m * __builtin_amdgcn_cosf(ph), m * __builtin_amdgcn_sinf(ph)}; };
  char* R0 = smem + w * 2 * K1_REG;
  char* R1 = R0 + K1_REG;
  auto put = [&](char* R, int row, cf v) { *(unsigned*)(R + row * 272 + lane * 4) = pkh(v[0], v[1]); };
  const int fi = lane & 15, fg = lane >> 4;
  auto frag = [&](const char* R, int ks) { return *(const h8v*)(R + fi * 272 + ks * 64 + fg * 16); };
  const cf W8 = E(8.f);
  cf w1 = E(1.f), E128 = E(128.f);
  w1[1] = -w1[1]; E128[1] = -E128[1];
  cf T0, T1, T2, T3;
  {
    put(R0, 0, cf{1.f, 0.f});
    put(R0, 1, W8);
    const cf W16 = cmul(W8, W8);
    cf F3, W32, F4, F5, F6, F7;
    cmul2(F3, W32, W16, W8, W16, W16);
    put(R0, 2, W16); put(R0, 3, F3);
    cmul4(F4, F5, F6, F7, cf{1.f, 0.f}, W8, W16, F3, W32);
    put(R0, 4, F4); put(R0, 5, F5); put(R0, 6, F6); put(R0, 7, F7);
    const cf W64 = cmul(W32, W32);
    cmul4(T0, T1, T2, T3, cf{1.f, 0.f}, W8, W16, F3, W64);
    put(R0, 8, T0); put(R0, 9, T1); put(R0, 10, T2); put(R0, 11, T3);
    cmul4(T0, T1, T2, T3, F4, F5, F6, F7, W64);
    put(R0, 12, T0); put(R0, 13, T1); put(R0, 14, T2); put(R0, 15, T3);
  }
  CFENCE();
  h8v fb[4];
#pragma unroll
  for (int ks = 0; ks < 4; ++ks) fb[ks] = frag(R0, ks);
  CFENCE();
  cf G[8];
  G[0] = cf{cr * gs, -ci * gs};
  {
    const cf w2 = cmul(w1, w1);
    cf w4;
    cmul2(G[1], w4, G[0], w1, w2, w2);
    cmul2(G[2], G[3], G[0], w2, G[1], w2);
    cmul4(G[4], G[5], G[6], G[7], G[0], G[1], G[2], G[3], w4);
  }
#pragma unroll
  for (int i = 0; i < 8; ++i) put(R1, i, G[i]);
  cmul4(T0, T1, T2, T3, G[0], G[1], G[2], G[3], E128);
  put(R1, 8, T0); put(R1, 9, T1); put(R1, 10, T2); put(R1, 11, T3);
  cmul4(T0, T1, T2, T3, G[4], G[5], G[6], G[7], E128);
  put(R1, 12, T0); put(R1, 13, T1); put(R1, 14, T2); put(R1, 15, T3);
  CFENCE();
  f4v acc0 = {0, 0, 0, 0}, acc1 = {0, 0, 0, 0}, acc2 = {0, 0, 0, 0};
#pragma unroll
  for (int ks = 0; ks < 4; ++ks) acc0 = __builtin_amdgcn_mfma_f32_16x16x32_f16(frag(R1, ks), fb[ks], acc0, 0, 0, 0);
  CFENCE();
  const cf E256 = cmul(E128, E128);
  cf E384, E512;
  cmul2(E384, E512, E256, E128, E256, E256);
  cf E640, E768, E896, E1024;
  cmul4(E640, E768, E896, E1024, E128, E256, E384, E512, E512);
  cmul4(T0, T1, T2, T3, G[0], G[2], G[4], G[6], E256);
  put(R0, 0, T0); put(R0, 1, T1); put(R0, 2, T2); put(R0, 3, T3);
  cmul4(T0, T1, T2, T3, G[0], G[2], G[4], G[6], E384);
  put(R0, 4, T0); put(R0, 5, T1); put(R0, 6, T2); put(R0, 7, T3);
  cmul4(T0, T1, T2, T3, E512, E640, E768, E896, G[0]);
  put(R0, 8, T0); put(R0, 10, T1); put(R0, 12, T2); put(R0, 14, T3);
  cmul4(T0, T1, T2, T3, E512, E640, E768, E896, G[4]);
  put(R0, 9, T0); put(R0, 11, T1); put(R0, 13, T2); put(R0, 15, T3);
  CFENCE();
#pragma unroll
  for (int ks = 0; ks < 4; ++ks) acc1 = __builtin_amdgcn_mfma_f32_16x16x32_f16(frag(R0, ks), fb[ks], acc1, 0, 0, 0);
  CFENCE();
  {
    const cf H0 = cmul(G[0], E1024);
    cf H1, H2, H3, H8;
    cmul4(H1, H2, H3, H8, E128, E256, E384, E1024, H0);
    put(R1, 0, H0); put(R1, 1, H1); put(R1, 2, H2); put(R1, 3, H3); put(R1, 8, H8);
    cmul4(T0, T1, T2, T3, E512, E640, E768, E896, H0);
    put(R1, 4, T0); put(R1, 5, T1); put(R1, 6, T2); put(R1, 7, T3);
  }
  CFENCE();
#pragma unroll
  for (int ks = 0; ks < 4; ++ks) acc2 = __builtin_amdgcn_mfma_f32_16x16x32_f16(frag(R1, ks), fb[ks], acc2, 0, 0, 0);
  CFENCE();
  {
    u16* tab = (u16*)R0;
    const int b = fi, g = fg;
    const unsigned p00 = pkh(acc0[0], acc0[1]), p01 = pkh(acc0[2], acc0[3]), p10 = pkh(acc1[0], acc1[1]), p11 = pkh(acc1[2], acc1[3]);
    const unsigned p20 = pkh(acc2[0], acc2[1]), p21 = pkh(acc2[2], acc2[3]);
    *(u2v*)(tab + 4 * (g & 1) + 128 * (g >> 1) + 8 * b) = u2v{p00, p01};
    if (g < 2) {
      *(u2v*)(tab + 256 + 64 * g + 4 * b) = u2v{p10, p11};
      tab[512 + 16 * (4 * g) + b] = (u16)p20; tab[512 + 16 * (4 * g + 1) + b] = (u16)(p20 >> 16);
      tab[512 + 16 * (4 * g + 2) + b] = (u16)p21; tab[512 + 16 * (4 * g + 3) + b] = (u16)(p21 >> 16);
    } else {
      *(unsigned*)(tab + 384 + 32 * (2 * (g - 2)) + 2 * b) = p10;
      *(unsigned*)(tab + 384 + 32 * (2 * (g - 2) + 1) + 2 * b) = p11;
      if (g == 2 && b < 8) *(unsigned*)(tab + 640 + 2 * b) = b == 0 ? (p20 & 0xffffu) : 0u;
    }
    if (lane < 6) *(u4v*)((char*)tab + 1312 + lane * 16) = u4v{(unsigned)ex, 0u, 0u, 0u};
    CFENCE();
    st16(16, ws + WS_KD, (unsigned)(unit * KDU + lane * 16), *(const u4v*)((const char*)tab + lane * 16));
    if (lane < 24) st16(16, ws + WS_KD, (unsigned)(unit * KDU + 1024 + lane * 16), *(const u4v*)((const char*)tab + 1024 + lane * 16));
  }
}

constexpr int K2_COPY = 1344, K2_TT = 4 * K2_COPY, K2_V3 = K2_TT + 6 * 768, K2_V5 = K2_V3 + 3616, K2_V7 = K2_V5 + 3616, K2_V9 = K2_V7 + 2048;
constexpr int K2_CH = K2_V9 + 2048;
constexpr int K2_UL = 4 * K2_CH;
constexpr int K2_KD = K2_UL + 4 * 4608;
constexpr int K2_YB = K2_KD + 4 * 1312;
constexpr int K2_XB = K2_YB + 16384;
constexpr int K2_SC = K2_XB + 16384;
constexpr int K2_LDS = K2_SC + 32;
#define K2_MAINLOOP_ASM \
  "ds_read_b64_tr_b16 v[64:65], %[kb] offset:8\n\t" \
  "ds_read_b64_tr_b16 v[66:67], %[kb] offset:0\n\t" \
  "ds_read_b128 v[32:35], %[ub] offset:448\n\t" \
  "ds_read_b128 v[36:39], %[ub] offset:960\n\t" \
  "ds_read_b128 v[40:43], %[ub] offset:1472\n\t" \
  "ds_read_b128 v[44:47], %[ub] offset:1984\n\t" \
  "ds_read_b128 v[48:51], %[ub] offset:2496\n\t" \
  "ds_read_b128 v[52:55], %[ub] offset:3008\n\t" \
  "ds_read_b128 v[56:59], %[ub] offset:3520\n\t" \
  "ds_read_b128 v[60:63], %[ub] offset:4032\n\t" \
  "ds_read_b64_tr_b16 v[68:69], %[kb] offset:520\n\t" \
  "ds_read_b64_tr_b16 v[70:71], %[kb] offset:512\n\t" \
  "ds_read_b64_tr_b16 v[112:113], %[kb] offset:1032\n\t" \
  "ds_read_b64_tr_b16 v[114:115], %[kb] offset:1024\n\t" \
  "ds_read_b64_tr_b16 v[116:117], %[kb] offset:1184\n\t" \
  "s_waitcnt lgkmcnt(13)\n\t" \
  "ds_read_b64_tr_b16 v[118:119], %[kb] offset:1176\n\t" \
  "s_waitcnt lgkmcnt(13)\n\t" \
  "v_mfma_f32_16x16x32_f16 v[0:3], v[64:67], v[32:35], v[0:3]\n\t" \
  "ds_read_b64_tr_b16 v[104:105], %[kb] offset:72\n\t" \
  "s_waitcnt lgkmcnt(13)\n\t" \
  "v_mfma_f32_16x16x32_f16 v[4:7], v[64:67], v[36:39], v[4:7]\n\t" \
  "ds_read_b64_tr_b16 v[106:107], %[kb] offset:64\n\t" \
  "s_waitcnt lgkmcnt(13)\n\t" \
  "v_mfma_f32_16x16x32_f16 v[8:11], v[64:67], v[40:43], v[8:11]\n\t" \
  "ds_read_b128 v[72:75], %[ub] offset:384\n\t" \
  "s_waitcnt lgkmcnt(13)\n\t" \
  "v_mfma_f32_16x16x32_f16 v[12:15], v[64:67], v[44:47], v[12:15]\n\t" \
  "ds_read_b128 v[76:79], %[ub] offset:896\n\t" \
  "s_waitcnt lgkmcnt(13)\n\t" \
  "v_mfma_f32_16x16x32_f16 v[16:19], v[64:67], v[48:51], v[16:19]\n\t" \
  "ds_read_b128 v[80:83], %[ub] offset:1408\n\t" \
  "s_waitcnt lgkmcnt(13)\n\t" \
  "v_mfma_f32_16x16x32_f16 v[20:23], v[64:67], v[52:55], v[20:23]\n\t" \
  "ds_read_b128 v[84:87], %[ub] offset:1920\n\t" \
  "s_waitcnt lgkmcnt(13)\n\t" \
  "v_mfma_f32_16x16x32_f16 v[24:27], v[64:67], v[56:59], v[24:27]\n\t" \
  "ds_read_b128 v[88:91], %[ub] offset:2432\n\t" \
  "s_waitcnt lgkmcnt(13)\n\t" \
  "v_mfma_f32_16x16x32_f16 v[28:31], v[64:67], v[60:63], v[28:31]\n\t" \
  "ds_read_b128 v[92:95], %[ub] offset:2944\n\t" \
  "s_waitcnt lgkmcnt(12)\n\t" \
  "v_mfma_f32_16x16x32_f16 v[4:7], v[68:71], v[32:35], v[4:7]\n\t" \
  "ds_read_b128 v[96:99], %[ub] offset:3456\n\t" \
  "v_mfma_f32_16x16x32_f16 v[8:11], v[68:71], v[36:39], v[8:11]\n\t" \
  "ds_read_b128 v[100:103], %[ub] offset:3968\n\t" \
  "v_mfma_f32_16x16x32_f16 v[12:15], v[68:71], v[40:43], v[12:15]\n\t" \
  "v_mfma_f32_16x16x32_f16 v[16:19], v[68:71], v[44:47], v[16:19]\n\t" \
  "v_mfma_f32_16x16x32_f16 v[20:23], v[68:71], v[48:51], v[20:23]\n\t" \
  "v_mfma_f32_16x16x32_f16 v[24:27], v[68:71], v[52:55], v[24:27]\n\t" \
  "v_mfma_f32_16x16x32_f16 v[28:31], v[68:71], v[56:59], v[28:31]\n\t" \
  "s_waitcnt lgkmcnt(12)\n\t" \
  "v_mfma_f32_16x16x32_f16 v[8:11], v[112:115], v[32:35], v[8:11]\n\t" \
  "ds_read_b64_tr_b16 v[108:109], %[kb] offset:584\n\t" \
  "v_mfma_f32_16x16x32_f16 v[12:15], v[112:115], v[36:39], v[12:15]\n\t" \
  "ds_read_b64_tr_b16 v[110:111], %[kb] offset:576\n\t" \
  "v_mfma_f32_16x16x32_f16 v[16:19], v[112:115], v[40:43], v[16:19]\n\t" \
  "v_mfma_f32_16x16x32_f16 v[20:23], v[112:115], v[44:47], v[20:23]\n\t" \
  "v_mfma_f32_16x16x32_f16 v[24:27], v[112:115], v[48:51], v[24:27]\n\t" \
  "v_mfma_f32_16x16x32_f16 v[28:31], v[112:115], v[52:55], v[28:31]\n\t" \
  "s_waitcnt lgkmcnt(12)\n\t" \
  "v_mfma_f32_16x16x32_f16 v[16:19], v[116:119], v[32:35], v[16:19]\n\t" \
  "v_mfma_f32_16x16x32_f16 v[20:23], v[116:119], v[36:39], v[20:23]\n\t" \
  "v_mfma_f32_16x16x32_f16 v[24:27], v[116:119], v[40:43], v[24:27]\n\t" \
  "v_mfma_f32_16x16x32_f16 v[28:31], v[116:119], v[44:47], v[28:31]\n\t" \
  "s_waitcnt lgkmcnt(9)\n\t" \
  "v_mfma_f32_16x16x32_f16 v[0:3], v[104:107], v[72:75], v[0:3]\n\t" \
  "ds_read_b64_tr_b16 v[64:65], %[kb] offset:136\n\t" \
  "s_waitcnt lgkmcnt(9)\n\t" \
  "v_mfma_f32_16x16x32_f16 v[4:7], v[104:107], v[76:79], v[4:7]\n\t" \
  "ds_read_b64_tr_b16 v[66:67], %[kb] offset:128\n\t" \
  "s_waitcnt lgkmcnt(9)\n\t" \
  "v_mfma_f32_16x16x32_f16 v[8:11], v[104:107], v[80:83], v[8:11]\n\t" \
  "ds_read_b128 v[32:35], %[ub] offset:320\n\t" \
  "s_waitcnt lgkmcnt(9)\n\t" \
  "v_mfma_f32_16x16x32_f16 v[12:15], v[104:107], v[84:87], v[12:15]\n\t" \
  "ds_read_b128 v[36:39], %[ub] offset:832\n\t" \
  "s_waitcnt lgkmcnt(9)\n\t" \
  "v_mfma_f32_16x16x32_f16 v[16:19], v[104:107], v[88:91], v[16:19]\n\t" \
  "ds_read_b128 v[40:43], %[ub] offset:1344\n\t" \
  "s_waitcnt lgkmcnt(9)\n\t" \
  "v_mfma_f32_16x16x32_f16 v[20:23], v[104:107], v[92:95], v[20:23]\n\t" \
  "ds_read_b128 v[44:47], %[ub] offset:1856\n\t" \
  "s_waitcnt lgkmcnt(9)\n\t" \
  "v_mfma_f32_16x16x32_f16 v[24:27], v[104:107], v[96:99], v[24:27]\n\t" \
  "ds_read_b128 v[48:51], %[ub] offset:2368\n\t" \
  "s_waitcnt lgkmcnt(9)\n\t" \
  "v_mfma_f32_16x16x32_f16 v[28:31], v[104:107], v[100:103], v[28:31]\n\t" \
  "ds_read_b128 v[52:55], %[ub] offset:2880\n\t" \
  "s_waitcnt lgkmcnt(8)\n\t" \
  "v_mfma_f32_16x16x32_f16 v[4:7], v[108:111], v[72:75], v[4:7]\n\t" \
  "ds_read_b128 v[56:59], %[ub] offset:3392\n\t" \
  "v_mfma_f32_16x16x32_f16 v[8:11], v[108:111], v[76:79], v[8:11]\n\t" \
  "ds_read_b128 v[60:63], %[ub] offset:3904\n\t" \
  "v_mfma_f32_16x16x32_f16 v[12:15], v[108:111], v[80:83], v[12:15]\n\t" \
  "ds_read_b64_tr_b16 v[68:69], %[kb] offset:648\n\t" \
  "v_mfma_f32_16x16x32_f16 v[16:19], v[108:111], v[84:87], v[16:19]\n\t" \
  "ds_read_b64_tr_b16 v[70:71], %[kb] offset:640\n\t" \
  "v_mfma_f32_16x16x32_f16 v[20:23], v[108:111], v[88:91], v[20:23]\n\t" \
  "v_mfma_f32_16x16x32_f16 v[24:27], v[108:111], v[92:95], v[24:27]\n\t" \
  "v_mfma_f32_16x16x32_f16 v[28:31], v[108:111], v[96:99], v[28:31]\n\t" \
  "s_waitcnt lgkmcnt(9)\n\t" \
  "v_mfma_f32_16x16x32_f16 v[0:3], v[64:67], v[32:35], v[0:3]\n\t" \
  "ds_read_b64_tr_b16 v[104:105], %[kb] offset:200\n\t" \
  "s_waitcnt lgkmcnt(9)\n\t" \
  "v_mfma_f32_16x16x32_f16 v[4:7], v[64:67], v[36:39], v[4:7]\n\t" \
  "ds_read_b64_tr_b16 v[106:107], %[kb] offset:192\n\t" \
  "s_waitcnt lgkmcnt(9)\n\t" \
  "v_mfma_f32_16x16x32_f16 v[8:11], v[64:67], v[40:43], v[8:11]\n\t" \
  "ds_read_b128 v[72:75], %[ub] offset:256\n\t" \
  "s_waitcnt lgkmcnt(9)\n\t" \
  "v_mfma_f32_16x16x32_f16 v[12:15], v[64:67], v[44:47], v[12:15]\n\t" \
  "ds_read_b128 v[76:79], %[ub] offset:768\n\t" \
  "s_waitcnt lgkmcnt(9)\n\t" \
  "v_mfma_f32_16x16x32_f16 v[16:19], v[64:67], v[48:51], v[16:19]\n\t" \
  "ds_read_b128 v[80:83], %[ub] offset:1280\n\t" \
  "s_waitcnt lgkmcnt(9)\n\t" \
  "v_mfma_f32_16x16x32_f16 v[20:23], v[64:67], v[52:55], v[20:23]\n\t" \
  "ds_read_b128 v[84:87], %[ub] offset:1792\n\t" \
  "s_waitcnt lgkmcnt(9)\n\t" \
  "v_mfma_f32_16x16x32_f16 v[24:27], v[64:67], v[56:59], v[24:27]\n\t" \
  "ds_read_b128 v[88:91], %[ub] offset:2304\n\t" \
  "s_waitcnt lgkmcnt(9)\n\t" \
  "v_mfma_f32_16x16x32_f16 v[28:31], v[64:67], v[60:63], v[28:31]\n\t" \
  "ds_read_b128 v[92:95], %[ub] offset:2816\n\t" \
  "s_waitcnt lgkmcnt(8)\n\t" \
  "v_mfma_f32_16x16x32_f16 v[4:7], v[68:71], v[32:35], v[4:7]\n\t" \
  "ds_read_b128 v[96:99], %[ub] offset:3328\n\t" \
  "v_mfma_f32_16x16x32_f16 v[8:11], v[68:71], v[36:39], v[8:11]\n\t" \
  "ds_read_b128 v[100:103], %[ub] offset:3840\n\t" \
  "v_mfma_f32_16x16x32_f16 v[12:15], v[68:71], v[40:43], v[12:15]\n\t" \
  "ds_read_b64_tr_b16 v[108:109], %[kb] offset:712\n\t" \
  "v_mfma_f32_16x16x32_f16 v[16:19], v[68:71], v[44:47], v[16:19]\n\t" \
  "ds_read_b64_tr_b16 v[110:111], %[kb] offset:704\n\t" \
  "v_mfma_f32_16x16x32_f16 v[20:23], v[68:71], v[48:51], v[20:23]\n\t" \
  "v_mfma_f32_16x16x32_f16 v[24:27], v[68:71], v[52:55], v[24:27]\n\t" \
  "v_mfma_f32_16x16x32_f16 v[28:31], v[68:71], v[56:59], v[28:31]\n\t" \
  "s_waitcnt lgkmcnt(9)\n\t" \
  "v_mfma_f32_16x16x32_f16 v[0:3], v[104:107], v[72:75], v[0:3]\n\t" \
  "ds_read_b64_tr_b16 v[64:65], %[kb] offset:264\n\t" \
  "s_waitcnt lgkmcnt(9)\n\t" \
  "v_mfma_f32_16x16x32_f16 v[4:7], v[104:107], v[76:79], v[4:7]\n\t" \
  "ds_read_b64_tr_b16 v[66:67], %[kb] offset:256\n\t" \
  "s_waitcnt lgkmcnt(9)\n\t" \
  "v_mfma_f32_16x16x32_f16 v[8:11], v[104:107], v[80:83], v[8:11]\n\t" \
  "ds_read_b128 v[32:35], %[ub] offset:192\n\t" \
  "s_waitcnt lgkmcnt(9)\n\t" \
  "v_mfma_f32_16x16x32_f16 v[12:15], v[104:107], v[84:87], v[12:15]\n\t" \
  "ds_read_b128 v[36:39], %[ub] offset:704\n\t" \
  "s_waitcnt lgkmcnt(9)\n\t" \
  "v_mfma_f32_16x16x32_f16 v[16:19], v[104:107], v[88:91], v[16:19]\n\t" \
  "ds_read_b128 v[40:43], %[ub] offset:1216\n\t" \
  "s_waitcnt lgkmcnt(9)\n\t" \
  "v_mfma_f32_16x16x32_f16 v[20:23], v[104:107], v[92:95], v[20:23]\n\t" \
  "ds_read_b128 v[44:47], %[ub] offset:1728\n\t" \
  "s_waitcnt lgkmcnt(9)\n\t" \
  "v_mfma_f32_16x16x32_f16 v[24:27], v[104:107], v[96:99], v[24:27]\n\t" \
  "ds_read_b128 v[48:51], %[ub] offset:2240\n\t" \
  "s_waitcnt lgkmcnt(9)\n\t" \
  "v_mfma_f32_16x16x32_f16 v[28:31], v[104:107], v[100:103], v[28:31]\n\t" \
  "ds_read_b128 v[52:55], %[ub] offset:2752\n\t" \
  "s_waitcnt lgkmcnt(8)\n\t" \
  "v_mfma_f32_16x16x32_f16 v[4:7], v[108:111], v[72:75], v[4:7]\n\t" \
  "ds_read_b128 v[56:59], %[ub] offset:3264\n\t" \
  "v_mfma_f32_16x16x32_f16 v[8:11], v[108:111], v[76:79], v[8:11]\n\t" \
  "ds_read_b128 v[60:63], %[ub] offset:3776\n\t" \
  "v_mfma_f32_16x16x32_f16 v[12:15], v[108:111], v[80:83], v[12:15]\n\t" \
  "ds_read_b64_tr_b16 v[68:69], %[kb] offset:776\n\t" \
  "v_mfma_f32_16x16x32_f16 v[16:19], v[108:111], v[84:87], v[16:19]\n\t" \
  "ds_read_b64_tr_b16 v[70:71], %[kb] offset:768\n\t" \
  "v_mfma_f32_16x16x32_f16 v[20:23], v[108:111], v[88:91], v[20:23]\n\t" \
  "v_mfma_f32_16x16x32_f16 v[24:27], v[108:111], v[92:95], v[24:27]\n\t" \
  "v_mfma_f32_16x16x32_f16 v[28:31], v[108:111], v[96:99], v[28:31]\n\t" \
  "s_waitcnt lgkmcnt(9)\n\t" \
  "v_mfma_f32_16x16x32_f16 v[0:3], v[64:67], v[32:35], v[0:3]\n\t" \
  "ds_read_b64_tr_b16 v[104:105], %[kb] offset:328\n\t" \
  "s_waitcnt lgkmcnt(9)\n\t" \
  "v_mfma_f32_16x16x32_f16 v[4:7], v[64:67], v[36:39], v[4:7]\n\t" \
  "ds_read_b64_tr_b16 v[106:107], %[kb] offset:320\n\t" \
  "s_waitcnt lgkmcnt(9)\n\t" \
  "v_mfma_f32_16x16x32_f16 v[8:11], v[64:67], v[40:43], v[8:11]\n\t" \
  "ds_read_b128 v[72:75], %[ub] offset:128\n\t" \
  "s_waitcnt lgkmcnt(9)\n\t" \
  "v_mfma_f32_16x16x32_f16 v[12:15], v[64:67], v[44:47], v[12:15]\n\t" \
  "ds_read_b128 v[76:79], %[ub] offset:640\n\t" \
  "s_waitcnt lgkmcnt(9)\n\t" \
  "v_mfma_f32_16x16x32_f16 v[16:19], v[64:67], v[48:51], v[16:19]\n\t" \
  "ds_read_b128 v[80:83], %[ub] offset:1152\n\t" \
  "s_waitcnt lgkmcnt(9)\n\t" \
  "v_mfma_f32_16x16x32_f16 v[20:23], v[64:67], v[52:55], v[20:23]\n\t" \
  "ds_read_b128 v[84:87], %[ub] offset:1664\n\t" \
  "s_waitcnt lgkmcnt(9)\n\t" \
  "v_mfma_f32_16x16x32_f16 v[24:27], v[64:67], v[56:59], v[24:27]\n\t" \
  "ds_read_b128 v[88:91], %[ub] offset:2176\n\t" \
  "s_waitcnt lgkmcnt(9)\n\t" \
  "v_mfma_f32_16x16x32_f16 v[28:31], v[64:67], v[60:63], v[28:31]\n\t" \
  "ds_read_b128 v[92:95], %[ub] offset:2688\n\t" \
  "s_waitcnt lgkmcnt(8)\n\t" \
  "v_mfma_f32_16x16x32_f16 v[4:7], v[68:71], v[32:35], v[4:7]\n\t" \
  "ds_read_b128 v[96:99], %[ub] offset:3200\n\t" \
  "v_mfma_f32_16x16x32_f16 v[8:11], v[68:71], v[36:39], v[8:11]\n\t" \
  "ds_read_b128 v[100:103], %[ub] offset:3712\n\t" \
  "v_mfma_f32_16x16x32_f16 v[12:15], v[68:71], v[40:43], v[12:15]\n\t" \
  "ds_read_b64_tr_b16 v[108:109], %[kb] offset:840\n\t" \
  "v_mfma_f32_16x16x32_f16 v[16:19], v[68:71], v[44:47], v[16:19]\n\t" \
  "ds_read_b64_tr_b16 v[110:111], %[kb] offset:832\n\t" \
  "v_mfma_f32_16x16x32_f16 v[20:23], v[68:71], v[48:51], v[20:23]\n\t" \
  "v_mfma_f32_16x16x32_f16 v[24:27], v[68:71], v[52:55], v[24:27]\n\t" \
  "v_mfma_f32_16x16x32_f16 v[28:31], v[68:71], v[56:59], v[28:31]\n\t" \
  "s_waitcnt lgkmcnt(9)\n\t" \
  "v_mfma_f32_16x16x32_f16 v[0:3], v[104:107], v[72:75], v[0:3]\n\t" \
  "ds_read_b64_tr_b16 v[64:65], %[kb] offset:392\n\t" \
  "s_waitcnt lgkmcnt(9)\n\t" \
  "v_mfma_f32_16x16x32_f16 v[4:7], v[104:107], v[76:79], v[4:7]\n\t" \
  "ds_read_b64_tr_b16 v[66:67], %[kb] offset:384\n\t" \
  "s_waitcnt lgkmcnt(9)\n\t" \
  "v_mfma_f32_16x16x32_f16 v[8:11], v[104:107], v[80:83], v[8:11]\n\t" \
  "ds_read_b128 v[32:35], %[ub] offset:64\n\t" \
  "s_waitcnt lgkmcnt(9)\n\t" \
  "v_mfma_f32_16x16x32_f16 v[12:15], v[104:107], v[84:87], v[12:15]\n\t" \
  "ds_read_b128 v[36:39], %[ub] offset:576\n\t" \
  "s_waitcnt lgkmcnt(9)\n\t" \
  "v_mfma_f32_16x16x32_f16 v[16:19], v[104:107], v[88:91], v[16:19]\n\t" \
  "ds_read_b128 v[40:43], %[ub] offset:1088\n\t" \
  "s_waitcnt lgkmcnt(9)\n\t" \
  "v_mfma_f32_16x16x32_f16 v[20:23], v[104:107], v[92:95], v[20:23]\n\t" \
  "ds_read_b128 v[44:47], %[ub] offset:1600\n\t" \
  "s_waitcnt lgkmcnt(9)\n\t" \
  "v_mfma_f32_16x16x32_f16 v[24:27], v[104:107], v[96:99], v[24:27]\n\t" \
  "ds_read_b128 v[48:51], %[ub] offset:2112\n\t" \
  "s_waitcnt lgkmcnt(9)\n\t" \
  "v_mfma_f32_16x16x32_f16 v[28:31], v[104:107], v[100:103], v[28:31]\n\t" \
  "ds_read_b128 v[52:55], %[ub] offset:2624\n\t" \
  "s_waitcnt lgkmcnt(8)\n\t" \
  "v_mfma_f32_16x16x32_f16 v[4:7], v[108:111], v[72:75], v[4:7]\n\t" \
  "ds_read_b128 v[56:59], %[ub] offset:3136\n\t" \
  "v_mfma_f32_16x16x32_f16 v[8:11], v[108:111], v[76:79], v[8:11]\n\t" \
  "ds_read_b128 v[60:63], %[ub] offset:3648\n\t" \
  "v_mfma_f32_16x16x32_f16 v[12:15], v[108:111], v[80:83], v[12:15]\n\t" \
  "ds_read_b64_tr_b16 v[68:69], %[kb] offset:904\n\t" \
  "v_mfma_f32_16x16x32_f16 v[16:19], v[108:111], v[84:87], v[16:19]\n\t" \
  "ds_read_b64_tr_b16 v[70:71], %[kb] offset:896\n\t" \
  "v_mfma_f32_16x16x32_f16 v[20:23], v[108:111], v[88:91], v[20:23]\n\t" \
  "v_mfma_f32_16x16x32_f16 v[24:27], v[108:111], v[92:95], v[24:27]\n\t" \
  "v_mfma_f32_16x16x32_f16 v[28:31], v[108:111], v[96:99], v[28:31]\n\t" \
  "s_waitcnt lgkmcnt(9)\n\t" \
  "v_mfma_f32_16x16x32_f16 v[0:3], v[64:67], v[32:35], v[0:3]\n\t" \
  "ds_read_b64_tr_b16 v[104:105], %[kb] offset:456\n\t" \
  "s_waitcnt lgkmcnt(9)\n\t" \
  "v_mfma_f32_16x16x32_f16 v[4:7], v[64:67], v[36:39], v[4:7]\n\t" \
  "ds_read_b64_tr_b16 v[106:107], %[kb] offset:448\n\t" \
  "s_waitcnt lgkmcnt(9)\n\t" \
  "v_mfma_f32_16x16x32_f16 v[8:11], v[64:67], v[40:43], v[8:11]\n\t" \
  "ds_read_b128 v[72:75], %[ub] offset:0\n\t" \
  "s_waitcnt lgkmcnt(9)\n\t" \
  "v_mfma_f32_16x16x32_f16 v[12:15], v[64:67], v[44:47], v[12:15]\n\t" \
  "ds_read_b128 v[76:79], %[ub] offset:512\n\t" \
  "s_waitcnt lgkmcnt(9)\n\t" \
  "v_mfma_f32_16x16x32_f16 v[16:19], v[64:67], v[48:51], v[16:19]\n\t" \
  "ds_read_b128 v[80:83], %[ub] offset:1024\n\t" \
  "s_waitcnt lgkmcnt(9)\n\t" \
  "v_mfma_f32_16x16x32_f16 v[20:23], v[64:67], v[52:55], v[20:23]\n\t" \
  "ds_read_b128 v[84:87], %[ub] offset:1536\n\t" \
  "s_waitcnt lgkmcnt(9)\n\t" \
  "v_mfma_f32_16x16x32_f16 v[24:27], v[64:67], v[56:59], v[24:27]\n\t" \
  "ds_read_b128 v[88:91], %[ub] offset:2048\n\t" \
  "s_waitcnt lgkmcnt(9)\n\t" \
  "v_mfma_f32_16x16x32_f16 v[28:31], v[64:67], v[60:63], v[28:31]\n\t" \
  "ds_read_b128 v[92:95], %[ub] offset:2560\n\t" \
  "s_waitcnt lgkmcnt(8)\n\t" \
  "v_mfma_f32_16x16x32_f16 v[4:7], v[68:71], v[32:35], v[4:7]\n\t" \
  "ds_read_b128 v[96:99], %[ub] offset:3072\n\t" \
  "v_mfma_f32_16x16x32_f16 v[8:11], v[68:71], v[36:39], v[8:11]\n\t" \
  "ds_read_b128 v[100:103], %[ub] offset:3584\n\t" \
  "v_mfma_f32_16x16x32_f16 v[12:15], v[68:71], v[40:43], v[12:15]\n\t" \
  "ds_read_b64_tr_b16 v[108:109], %[kb] offset:968\n\t" \
  "v_mfma_f32_16x16x32_f16 v[16:19], v[68:71], v[44:47], v[16:19]\n\t" \
  "ds_read_b64_tr_b16 v[110:111], %[kb] offset:960\n\t" \
  "v_mfma_f32_16x16x32_f16 v[20:23], v[68:71], v[48:51], v[20:23]\n\t" \
  "ds_read_b64_tr_b16 v[112:113], %[kb] offset:1120\n\t" \
  "v_mfma_f32_16x16x32_f16 v[24:27], v[68:71], v[52:55], v[24:27]\n\t" \
  "ds_read_b64_tr_b16 v[114:115], %[kb] offset:1112\n\t" \
  "v_mfma_f32_16x16x32_f16 v[28:31], v[68:71], v[56:59], v[28:31]\n\t" \
  "s_waitcnt lgkmcnt(0)\n\t" \
  "K2POLL_%=:\n\t" \
  "ds_read_b32 v116, %[fl]\n\t" \
  "s_waitcnt lgkmcnt(0)\n\t" \
  "v_readfirstlane_b32 %[tmp], v116\n\t" \
  "s_nop 0\n\t" \
  "s_cmp_eq_u32 %[tmp], 0\n\t" \
  "s_cbranch_scc1 K2POLL_%=\n\t" \
  "v_mfma_f32_16x16x32_f16 v[0:3], v[104:107], v[72:75], v[0:3]\n\t" \
  "ds_read_b64_tr_b16 v[32:33], %[tb] offset:64\n\t" \
  "v_mfma_f32_16x16x32_f16 v[4:7], v[104:107], v[76:79], v[4:7]\n\t" \
  "ds_read_b64_tr_b16 v[34:35], %[tb] offset:32\n\t" \
  "v_mfma_f32_16x16x32_f16 v[8:11], v[104:107], v[80:83], v[8:11]\n\t" \
  "ds_read_b64_tr_b16 v[36:37], %[vl] offset:3616\n\t" \
  "v_mfma_f32_16x16x32_f16 v[12:15], v[104:107], v[84:87], v[12:15]\n\t" \
  "ds_read_b64_tr_b16 v[38:39], %[vl] offset:4128\n\t" \
  "v_mfma_f32_16x16x32_f16 v[16:19], v[104:107], v[88:91], v[16:19]\n\t" \
  "ds_read_b64_tr_b16 v[40:41], %[tb] offset:96\n\t" \
  "v_mfma_f32_16x16x32_f16 v[20:23], v[104:107], v[92:95], v[20:23]\n\t" \
  "ds_read_b64_tr_b16 v[42:43], %[tb] offset:64\n\t" \
  "v_mfma_f32_16x16x32_f16 v[24:27], v[104:107], v[96:99], v[24:27]\n\t" \
  "ds_read_b64_tr_b16 v[44:45], %[tb] offset:128\n\t" \
  "v_mfma_f32_16x16x32_f16 v[28:31], v[104:107], v[100:103], v[28:31]\n\t" \
  "ds_read_b64_tr_b16 v[46:47], %[tb] offset:96\n\t" \
  "v_mfma_f32_16x16x32_f16 v[4:7], v[108:111], v[72:75], v[4:7]\n\t" \
  "ds_read_b64_tr_b16 v[48:49], %[vl] offset:4640\n\t" \
  "v_mfma_f32_16x16x32_f16 v[8:11], v[108:111], v[76:79], v[8:11]\n\t" \
  "ds_read_b64_tr_b16 v[50:51], %[vl] offset:5152\n\t" \
  "v_mfma_f32_16x16x32_f16 v[12:15], v[108:111], v[80:83], v[12:15]\n\t" \
  "ds_read_b64_tr_b16 v[52:53], %[vl] offset:4128\n\t" \
  "v_mfma_f32_16x16x32_f16 v[16:19], v[108:111], v[84:87], v[16:19]\n\t" \
  "ds_read_b64_tr_b16 v[54:55], %[vl] offset:4640\n\t" \
  "v_mfma_f32_16x16x32_f16 v[20:23], v[108:111], v[88:91], v[20:23]\n\t" \
  "ds_read_b64_tr_b16 v[56:57], %[vl] offset:5152\n\t" \
  "v_mfma_f32_16x16x32_f16 v[24:27], v[108:111], v[92:95], v[24:27]\n\t" \
  "ds_read_b64_tr_b16 v[58:59], %[vl] offset:5664\n\t" \
  "v_mfma_f32_16x16x32_f16 v[28:31], v[108:111], v[96:99], v[28:31]\n\t" \
  "v_mfma_f32_16x16x32_f16 v[12:15], v[112:115], v[72:75], v[12:15]\n\t" \
  "v_mfma_f32_16x16x32_f16 v[16:19], v[112:115], v[76:79], v[16:19]\n\t" \
  "v_mfma_f32_16x16x32_f16 v[20:23], v[112:115], v[80:83], v[20:23]\n\t" \
  "v_mfma_f32_16x16x32_f16 v[24:27], v[112:115], v[84:87], v[24:27]\n\t" \
  "v_mfma_f32_16x16x32_f16 v[28:31], v[112:115], v[88:91], v[28:31]\n\t" \
  "ds_read_b64_tr_b16 v[60:61], %[vl] offset:5664\n\t" \
  "s_waitcnt lgkmcnt(0)\n\t" \
  "ds_read_b64_tr_b16 v[62:63], %[vl] offset:6176\n\t" \
  "ds_read_b64_tr_b16 v[64:65], %[vl] offset:6176\n\t" \
  "ds_read_b64_tr_b16 v[66:67], %[vl] offset:6688\n\t" \
  "v_mfma_f32_16x16x32_f16 v[8:11], v[36:39], v[32:35], v[8:11]\n\t" \
  "ds_read_b64_tr_b16 v[72:73], %[tb] offset:832\n\t" \
  "ds_read_b64_tr_b16 v[74:75], %[tb] offset:800\n\t" \
  "v_mfma_f32_16x16x32_f16 v[12:15], v[36:39], v[40:43], v[12:15]\n\t" \
  "ds_read_b64_tr_b16 v[76:77], %[vl] offset:24\n\t" \
  "ds_read_b64_tr_b16 v[78:79], %[vl] offset:536\n\t" \
  "v_mfma_f32_16x16x32_f16 v[16:19], v[36:39], v[44:47], v[16:19]\n\t" \
  "ds_read_b64_tr_b16 v[80:81], %[tb] offset:864\n\t" \
  "ds_read_b64_tr_b16 v[82:83], %[tb] offset:832\n\t" \
  "v_mfma_f32_16x16x32_f16 v[16:19], v[48:51], v[32:35], v[16:19]\n\t" \
  "ds_read_b64_tr_b16 v[84:85], %[tb] offset:896\n\t" \
  "ds_read_b64_tr_b16 v[86:87], %[tb] offset:864\n\t" \
  "v_mfma_f32_16x16x32_f16 v[20:23], v[52:55], v[44:47], v[20:23]\n\t" \
  "ds_read_b64_tr_b16 v[88:89], %[vl] offset:1048\n\t" \
  "ds_read_b64_tr_b16 v[90:91], %[vl] offset:1560\n\t" \
  "v_mfma_f32_16x16x32_f16 v[20:23], v[56:59], v[32:35], v[20:23]\n\t" \
  "ds_read_b64_tr_b16 v[92:93], %[vl] offset:536\n\t" \
  "v_mfma_f32_16x16x32_f16 v[24:27], v[48:51], v[44:47], v[24:27]\n\t" \
  "s_waitcnt lgkmcnt(13)\n\t" \
  "v_mfma_f32_16x16x32_f16 v[24:27], v[60:63], v[32:35], v[24:27]\n\t" \
  "ds_read_b64_tr_b16 v[94:95], %[vl] offset:1048\n\t" \
  "v_mfma_f32_16x16x32_f16 v[28:31], v[56:59], v[44:47], v[28:31]\n\t" \
  "s_waitcnt lgkmcnt(12)\n\t" \
  "v_mfma_f32_16x16x32_f16 v[28:31], v[64:67], v[32:35], v[28:31]\n\t" \
  "ds_read_b64_tr_b16 v[96:97], %[vl] offset:1560\n\t" \
  "ds_read_b64_tr_b16 v[98:99], %[vl] offset:2072\n\t" \
  "ds_read_b64_tr_b16 v[100:101], %[vl] offset:2072\n\t" \
  "s_waitcnt lgkmcnt(0)\n\t" \
  "ds_read_b64_tr_b16 v[102:103], %[vl] offset:2584\n\t" \
  "ds_read_b64_tr_b16 v[104:105], %[vl] offset:2584\n\t" \
  "ds_read_b64_tr_b16 v[106:107], %[vl] offset:3096\n\t" \
  "v_mfma_f32_16x16x32_f16 v[8:11], v[76:79], v[72:75], v[8:11]\n\t" \
  "ds_read_b64_tr_b16 v[32:33], %[tb] offset:1600\n\t" \
  "ds_read_b64_tr_b16 v[34:35], %[tb] offset:1568\n\t" \
  "v_mfma_f32_16x16x32_f16 v[12:15], v[76:79], v[80:83], v[12:15]\n\t" \
  "ds_read_b64_tr_b16 v[36:37], %[vl] offset:3632\n\t" \
  "ds_read_b64_tr_b16 v[38:39], %[vl] offset:4144\n\t" \
  "v_mfma_f32_16x16x32_f16 v[16:19], v[76:79], v[84:87], v[16:19]\n\t" \
  "ds_read_b64_tr_b16 v[40:41], %[tb] offset:1632\n\t" \
  "ds_read_b64_tr_b16 v[42:43], %[tb] offset:1600\n\t" \
  "v_mfma_f32_16x16x32_f16 v[16:19], v[88:91], v[72:75], v[16:19]\n\t" \
  "ds_read_b64_tr_b16 v[44:45], %[tb] offset:1664\n\t" \
  "ds_read_b64_tr_b16 v[46:47], %[tb] offset:1632\n\t" \
  "v_mfma_f32_16x16x32_f16 v[20:23], v[92:95], v[84:87], v[20:23]\n\t" \
  "ds_read_b64_tr_b16 v[48:49], %[vl] offset:4656\n\t" \
  "ds_read_b64_tr_b16 v[50:51], %[vl] offset:5168\n\t" \
  "v_mfma_f32_16x16x32_f16 v[20:23], v[96:99], v[72:75], v[20:23]\n\t" \
  "ds_read_b64_tr_b16 v[52:53], %[vl] offset:4144\n\t" \
  "v_mfma_f32_16x16x32_f16 v[24:27], v[88:91], v[84:87], v[24:27]\n\t" \
  "s_waitcnt lgkmcnt(13)\n\t" \
  "v_mfma_f32_16x16x32_f16 v[24:27], v[100:103], v[72:75], v[24:27]\n\t" \
  "ds_read_b64_tr_b16 v[54:55], %[vl] offset:4656\n\t" \
  "v_mfma_f32_16x16x32_f16 v[28:31], v[96:99], v[84:87], v[28:31]\n\t" \
  "s_waitcnt lgkmcnt(12)\n\t" \
  "v_mfma_f32_16x16x32_f16 v[28:31], v[104:107], v[72:75], v[28:31]\n\t" \
  "ds_read_b64_tr_b16 v[56:57], %[vl] offset:5168\n\t" \
  "ds_read_b64_tr_b16 v[58:59], %[vl] offset:5680\n\t" \
  "ds_read_b64_tr_b16 v[60:61], %[vl] offset:5680\n\t" \
  "s_waitcnt lgkmcnt(0)\n\t" \
  "ds_read_b64_tr_b16 v[62:63], %[vl] offset:6192\n\t" \
  "ds_read_b64_tr_b16 v[64:65], %[vl] offset:6192\n\t" \
  "ds_read_b64_tr_b16 v[66:67], %[vl] offset:6704\n\t" \
  "v_mfma_f32_16x16x32_f16 v[8:11], v[36:39], v[32:35], v[8:11]\n\t" \
  "ds_read_b64_tr_b16 v[72:73], %[tb] offset:2368\n\t" \
  "ds_read_b64_tr_b16 v[74:75], %[tb] offset:2336\n\t" \
  "v_mfma_f32_16x16x32_f16 v[12:15], v[36:39], v[40:43], v[12:15]\n\t" \
  "ds_read_b64_tr_b16 v[76:77], %[vl] offset:8\n\t" \
  "ds_read_b64_tr_b16 v[78:79], %[vl] offset:520\n\t" \
  "v_mfma_f32_16x16x32_f16 v[16:19], v[36:39], v[44:47], v[16:19]\n\t" \
  "ds_read_b64_tr_b16 v[80:81], %[tb] offset:2400\n\t" \
  "ds_read_b64_tr_b16 v[82:83], %[tb] offset:2368\n\t" \
  "v_mfma_f32_16x16x32_f16 v[16:19], v[48:51], v[32:35], v[16:19]\n\t" \
  "ds_read_b64_tr_b16 v[84:85], %[tb] offset:2432\n\t" \
  "ds_read_b64_tr_b16 v[86:87], %[tb] offset:2400\n\t" \
  "v_mfma_f32_16x16x32_f16 v[20:23], v[52:55], v[44:47], v[20:23]\n\t" \
  "ds_read_b64_tr_b16 v[88:89], %[vl] offset:1032\n\t" \
  "ds_read_b64_tr_b16 v[90:91], %[vl] offset:1544\n\t" \
  "v_mfma_f32_16x16x32_f16 v[20:23], v[56:59], v[32:35], v[20:23]\n\t" \
  "ds_read_b64_tr_b16 v[92:93], %[vl] offset:520\n\t" \
  "v_mfma_f32_16x16x32_f16 v[24:27], v[48:51], v[44:47], v[24:27]\n\t" \
  "s_waitcnt lgkmcnt(13)\n\t" \
  "v_mfma_f32_16x16x32_f16 v[24:27], v[60:63], v[32:35], v[24:27]\n\t" \
  "ds_read_b64_tr_b16 v[94:95], %[vl] offset:1032\n\t" \
  "v_mfma_f32_16x16x32_f16 v[28:31], v[56:59], v[44:47], v[28:31]\n\t" \
  "s_waitcnt lgkmcnt(12)\n\t" \
  "v_mfma_f32_16x16x32_f16 v[28:31], v[64:67], v[32:35], v[28:31]\n\t" \
  "ds_read_b64_tr_b16 v[96:97], %[vl] offset:1544\n\t" \
  "ds_read_b64_tr_b16 v[98:99], %[vl] offset:2056\n\t" \
  "ds_read_b64_tr_b16 v[100:101], %[vl] offset:2056\n\t" \
  "s_waitcnt lgkmcnt(0)\n\t" \
  "ds_read_b64_tr_b16 v[102:103], %[vl] offset:2568\n\t" \
  "ds_read_b64_tr_b16 v[104:105], %[vl] offset:2568\n\t" \
  "ds_read_b64_tr_b16 v[106:107], %[vl] offset:3080\n\t" \
  "v_mfma_f32_16x16x32_f16 v[8:11], v[76:79], v[72:75], v[8:11]\n\t" \
  "ds_read_b64_tr_b16 v[32:33], %[tb] offset:3136\n\t" \
  "ds_read_b64_tr_b16 v[34:35], %[tb] offset:3104\n\t" \
  "v_mfma_f32_16x16x32_f16 v[12:15], v[76:79], v[80:83], v[12:15]\n\t" \
  "ds_read_b64_tr_b16 v[36:37], %[vl] offset:9280\n\t" \
  "ds_read_b64_tr_b16 v[38:39], %[vl] offset:9792\n\t" \
  "v_mfma_f32_16x16x32_f16 v[16:19], v[76:79], v[84:87], v[16:19]\n\t" \
  "ds_read_b64_tr_b16 v[40:41], %[tb] offset:3168\n\t" \
  "v_mfma_f32_16x16x32_f16 v[16:19], v[88:91], v[72:75], v[16:19]\n\t" \
  "ds_read_b64_tr_b16 v[42:43], %[tb] offset:3136\n\t" \
  "v_mfma_f32_16x16x32_f16 v[20:23], v[92:95], v[84:87], v[20:23]\n\t" \
  "ds_read_b64_tr_b16 v[44:45], %[tb] offset:3200\n\t" \
  "v_mfma_f32_16x16x32_f16 v[20:23], v[96:99], v[72:75], v[20:23]\n\t" \
  "ds_read_b64_tr_b16 v[46:47], %[tb] offset:3168\n\t" \
  "v_mfma_f32_16x16x32_f16 v[24:27], v[88:91], v[84:87], v[24:27]\n\t" \
  "ds_read_b64_tr_b16 v[48:49], %[vl] offset:10304\n\t" \
  "s_waitcnt lgkmcnt(11)\n\t" \
  "v_mfma_f32_16x16x32_f16 v[24:27], v[100:103], v[72:75], v[24:27]\n\t" \
  "ds_read_b64_tr_b16 v[50:51], %[vl] offset:10816\n\t" \
  "v_mfma_f32_16x16x32_f16 v[28:31], v[96:99], v[84:87], v[28:31]\n\t" \
  "ds_read_b64_tr_b16 v[52:53], %[tb] offset:3232\n\t" \
  "s_waitcnt lgkmcnt(11)\n\t" \
  "v_mfma_f32_16x16x32_f16 v[28:31], v[104:107], v[72:75], v[28:31]\n\t" \
  "ds_read_b64_tr_b16 v[54:55], %[tb] offset:3200\n\t" \
  "s_waitcnt lgkmcnt(8)\n\t" \
  "v_mfma_f32_16x16x32_f16 v[16:19], v[36:39], v[32:35], v[16:19]\n\t" \
  "ds_read_b64_tr_b16 v[72:73], %[tb] offset:3904\n\t" \
  "ds_read_b64_tr_b16 v[74:75], %[tb] offset:3872\n\t" \
  "s_waitcnt lgkmcnt(8)\n\t" \
  "v_mfma_f32_16x16x32_f16 v[20:23], v[36:39], v[40:43], v[20:23]\n\t" \
  "ds_read_b64_tr_b16 v[76:77], %[vl] offset:7232\n\t" \
  "ds_read_b64_tr_b16 v[78:79], %[vl] offset:7744\n\t" \
  "s_waitcnt lgkmcnt(8)\n\t" \
  "v_mfma_f32_16x16x32_f16 v[24:27], v[36:39], v[44:47], v[24:27]\n\t" \
  "ds_read_b64_tr_b16 v[80:81], %[tb] offset:3936\n\t" \
  "ds_read_b64_tr_b16 v[82:83], %[tb] offset:3904\n\t" \
  "s_waitcnt lgkmcnt(8)\n\t" \
  "v_mfma_f32_16x16x32_f16 v[24:27], v[48:51], v[32:35], v[24:27]\n\t" \
  "ds_read_b64_tr_b16 v[84:85], %[tb] offset:3968\n\t" \
  "ds_read_b64_tr_b16 v[86:87], %[tb] offset:3936\n\t" \
  "s_waitcnt lgkmcnt(8)\n\t" \
  "v_mfma_f32_16x16x32_f16 v[28:31], v[36:39], v[52:55], v[28:31]\n\t" \
  "ds_read_b64_tr_b16 v[88:89], %[vl] offset:8256\n\t" \
  "ds_read_b64_tr_b16 v[90:91], %[vl] offset:8768\n\t" \
  "v_mfma_f32_16x16x32_f16 v[28:31], v[48:51], v[40:43], v[28:31]\n\t" \
  "ds_read_b64_tr_b16 v[92:93], %[tb] offset:4000\n\t" \
  "ds_read_b64_tr_b16 v[94:95], %[tb] offset:3968\n\t" \
  "s_waitcnt lgkmcnt(8)\n\t" \
  "v_mfma_f32_16x16x32_f16 v[16:19], v[76:79], v[72:75], v[16:19]\n\t" \
  "s_waitcnt lgkmcnt(6)\n\t" \
  "v_mfma_f32_16x16x32_f16 v[20:23], v[76:79], v[80:83], v[20:23]\n\t" \
  "s_waitcnt lgkmcnt(4)\n\t" \
  "v_mfma_f32_16x16x32_f16 v[24:27], v[76:79], v[84:87], v[24:27]\n\t" \
  "s_waitcnt lgkmcnt(2)\n\t" \
  "v_mfma_f32_16x16x32_f16 v[24:27], v[88:91], v[72:75], v[24:27]\n\t" \
  "s_waitcnt lgkmcnt(0)\n\t" \
  "v_mfma_f32_16x16x32_f16 v[28:31], v[76:79], v[92:95], v[28:31]\n\t" \
  "v_mfma_f32_16x16x32_f16 v[28:31], v[88:91], v[80:83], v[28:31]\n\t" \
  "s_nop 7\n\t" \
  ""
__global__ __launch_bounds__(512) void k2_conv(const u16* __restrict__ Dskip, const u16* __restrict__ Wg, const u16* __restrict__ Wu,
                                               const u16* __restrict__ Wd, char* __restrict__ ws) {
  extern __shared__ __attribute__((aligned(16))) char smem[];
  const int tid = threadIdx.x, w = __builtin_amdgcn_readfirstlane(tid >> 6), lane = tid & 63;
  const int blk = blockIdx.x;
  if (blk >= 160) {
    const int hx = blk & 7, hj = (blk - 160) >> 3;
    if (hj >= 6) {
      const int ks = hx * 6 + hj - 6;
      constexpr int RS = 1280 + 32;
      u4v v[5];
#pragma unroll
      for (int i = 0; i < 5; ++i) { const int c = tid + 512 * i, r = c / 80; v[i] = *(const u4v*)(Wd + (size_t)(32 * ks + r) * DM + (c - r * 80) * 8); }
#pragma unroll
      for (int i = 0; i < 5; ++i) { const int c = tid + 512 * i, r = c / 80; *(u4v*)(smem + r * RS + (c - r * 80) * 16) = v[i]; }
      __syncthreads();
      const int g = lane >> 4, q = (lane >> 2) & 3, p = lane & 3;
#pragma unroll
      for (int it = 0; it < 5; ++it) {
        const int ctg = w * 5 + it;
        const char* a = smem + (8 * g + q) * RS + (16 * ctg + 4 * p) * 2;
        u2v a0 = __builtin_bit_cast(u2v, trread(a)), a1 = __builtin_bit_cast(u2v, trread(a + 4 * RS));
        const int cb = ctg / 5, ct = ctg - 5 * cb;
        st16(AUX_W2T, ws + WS_W2T, (unsigned)((((cb * 48 + ks) * 5 + ct) * 64 + lane) * 16), u4v{a0[0], a0[1], a1[0], a1[1]});
      }
    } else {
      const int m = hj / 3, c0 = 384 * (hx >> 1) + 128 * (hj - 3 * m);
      const u16* W = m ? Wu : Wg;
      const int cc = tid & 15, rg = tid >> 4;
      u4v v[20];
#pragma unroll
      for (int i = 0; i < 20; ++i) v[i] = *(const u4v*)(W + (size_t)(rg + 32 * i) * FF + c0 + cc * 8);
      float s[8] = {0, 0, 0, 0, 0, 0, 0, 0};
#pragma unroll
      for (int i = 0; i < 20; ++i)
#pragma unroll
        for (int j = 0; j < 4; ++j) { s[2 * j] += bflo(v[i][j]); s[2 * j + 1] += bfhi(v[i][j]); }
      float* red = (float*)smem;
#pragma unroll
      for (int j = 0; j < 8; ++j) red[rg * 129 + cc * 8 + j] = s[j];
      __syncthreads();
      if (tid < 128 && !(hx & 1)) {
        float t = 0.f;
#pragma unroll 8
        for (int r = 0; r < 32; ++r) t += red[r * 129 + tid];
        *(float*)(ws + WS_CS + (size_t)(m * FF + c0 + tid) * 4) = t;
      }
    }
    return;
  }
  const int cg = (blk & 7) * 20 + (blk >> 3);
  const char* ut = ws + WS_UT + (size_t)(cg >> 1) * 32768 + (cg & 1) * 64 + (tid >> 2) * 128 + (tid & 3) * 16;
  const u4v uv0 = *(const u4v*)ut, uv1 = *(const u4v*)(ut + 16384);
  const int kt = min(tid, 327), kc = kt / 82, kp = kt - 82 * kc;
  u4v kv3[3];
  int kex[3];
#pragma unroll
  for (int m = 0; m < 3; ++m) {
    kv3[m] = *(const u4v*)(ws + WS_KD + (size_t)((4 * cg + kc) * 3 + m) * KDU + kp * 16);
    kex[m] = *(const int*)(ws + WS_KD + (size_t)((4 * cg + kc) * 3 + m) * KDU + KDS * 2);
  }
  const int ht = tid & 255;
  const int etask = min(tid, 411), ech = (etask * 637) >> 16;
  const unsigned* tp0 = k2_tab + (etask - 103 * ech) * 8;
  u4v cst[2] = {*(const u4v*)tp0, *(const u4v*)(tp0 + 4)};
  u4v bandv[4];
#pragma unroll
  for (int h = 0; h < 4; ++h) bandv[h] = *(const u4v*)(k2_tab + K2T_BAND + h * 256 + lane * 4);
  const int zc = (min(tid, 163) * 1599) >> 16;
  const unsigned ztk = k2_tab[K2T_ZERO + min(tid, 163) - 41 * zc];
  u2v dsk = *(const u2v*)(Dskip + 4 * cg);
  __builtin_amdgcn_sched_barrier(0);
  if (tid < 128) *(u4v*)(smem + K2_UL + (tid >> 5) * 4608 + (tid & 31) * 16) = u4v{0, 0, 0, 0};
  else if (tid >= 144 && tid < 148) *(unsigned*)(smem + K2_SC + 16 + (tid - 144) * 4) = 0u;
  else if (tid < 144) { const int t = tid - 128; *(u4v*)(smem + (t >> 2) * K2_CH + ((t & 2) ? K2_V5 : K2_V3) + 3584 + (t & 1) * 16) = u4v{0, 0, 0, 0}; }
  *(u4v*)(smem + K2_UL + (tid & 3) * 4608 + (256 + 8 * (tid >> 2)) * 2) = uv0;
  *(u4v*)(smem + K2_UL + (tid & 3) * 4608 + (256 + 1024 + 8 * (tid >> 2)) * 2) = uv1;
  {
    const int em = max(kex[0], max(kex[1], kex[2]));
    u4v o;
#pragma unroll
    for (int j = 0; j < 4; ++j) {
      f2v a = {0.f, 0.f};
#pragma unroll
      for (int m = 0; m < 3; ++m) {
        const float f = __builtin_bit_cast(float, (unsigned)max(127 - 3 + kex[m] - em, 1) << 23);
        const unsigned e = kv3[m][j];
        a += f2v{hlo(e), hhi(e)} * f;
      }
      o[j] = pkh(a[0], a[1]);
    }
    if (tid < 328) *(u4v*)(smem + K2_KD + tid * 16) = o;
    if (tid < 328 && kp == 0) *(unsigned*)(smem + K2_SC + kc * 4) = (unsigned)(em + 1 + 4) << 23;
  }
  if (tid < 164) {
#pragma unroll
    for (int q = 0; q < 4; ++q) *(u4v*)(smem + zc * K2_CH + (ztk & 0xffff) + q * (ztk >> 16)) = u4v{0, 0, 0, 0};
  }
  asm volatile("" : "+v"(dsk), "+v"(cst[0]), "+v"(cst[1]), "+v"(bandv[0]), "+v"(bandv[1]), "+v"(bandv[2]), "+v"(bandv[3]));
  __syncthreads();
  const int c = w & 3, g = lane >> 4, q4 = (lane >> 2) & 3, p4 = lane & 3, i16 = lane & 15;
  if (tid < 412) {
    const char* kd = smem + K2_KD + ech * 1312;
    unsigned v[11];
#pragma unroll
    for (int e = 0; e < 11; ++e) {
      const unsigned wd = e < 8 ? cst[0][e >> 1] : cst[1][(e - 8) >> 1];
      v[e] = *(const u16*)(kd + ((e & 1) ? wd >> 16 : wd & 0xffff));
    }
    unsigned P[10];
#pragma unroll
    for (int e = 0; e < 10; ++e) P[e] = v[e] | (v[e + 1] << 16);
    const unsigned t11 = cst[1][1], t12 = cst[1][2];
    char* dst = smem + ech * K2_CH + (t11 >> 16);
    const unsigned estr = t12 & 0xffff;
#pragma unroll
    for (int q = 0; q < 4; ++q) *(u4v*)(dst + q * estr) = u4v{P[3 - q], P[5 - q], P[7 - q], P[9 - q]};
  }
  const unsigned cb0 = (unsigned)(size_t)(LDS_AS char*)smem + c * K2_CH, ul0 = (unsigned)(size_t)(LDS_AS char*)smem + K2_UL + c * 4608;
  unsigned kb = cb0 + q4 * K2_COPY + 2 * (32 - 8 * g + 4 * p4) - 8;
  unsigned ub = ul0 + 2 * (240 + 16 * i16 + 8 * g) - 448;
  unsigned tb = cb0 + K2_TT + q4 * 192 + 2 * (4 * p4 - 4 * g);
  unsigned vl = cb0 + K2_V3 + 32 * (4 * g + q4) + 8 * p4;

  f4v acc[8];
#pragma unroll
  for (int i = 0; i < 8; ++i) acc[i] = f4v{0, 0, 0, 0};
  asm volatile("" : "+v"(kb), "+v"(ub), "+v"(tb), "+v"(vl), "+{v[0:3]}"(acc[0]), "+{v[4:7]}"(acc[1]), "+{v[8:11]}"(acc[2]), "+{v[12:15]}"(acc[3]),
                    "+{v[16:19]}"(acc[4]), "+{v[20:23]}"(acc[5]), "+{v[24:27]}"(acc[6]), "+{v[28:31]}"(acc[7]));
  asm volatile("s_waitcnt lgkmcnt(0)" ::: "memory");
  __builtin_amdgcn_s_barrier();
  CFENCE();
  if (w < 4) {
    unsigned ptmp;
    const unsigned fl = (unsigned)(size_t)(LDS_AS char*)smem + K2_SC + 16 + c * 4;
    asm volatile(K2_MAINLOOP_ASM
                 : "+{v[0:3]}"(acc[0]), "+{v[4:7]}"(acc[1]), "+{v[8:11]}"(acc[2]), "+{v[12:15]}"(acc[3]), "+{v[16:19]}"(acc[4]),
                   "+{v[20:23]}"(acc[5]), "+{v[24:27]}"(acc[6]), "+{v[28:31]}"(acc[7]), [tmp] "=&s"(ptmp)
                 : [kb] "v"(kb), [ub] "v"(ub), [tb] "v"(tb), [vl] "v"(vl), [fl] "v"(fl)
                 : "memory", "scc", "v32", "v33", "v34", "v35", "v36", "v37", "v38", "v39", "v40", "v41", "v42", "v43", "v44", "v45", "v46", "v47", "v48", "v49", "v50", "v51", "v52", "v53", "v54", "v55", "v56", "v57", "v58", "v59", "v60", "v61", "v62", "v63", "v64", "v65", "v66", "v67", "v68", "v69", "v70", "v71", "v72", "v73", "v74", "v75", "v76", "v77", "v78", "v79", "v80", "v81", "v82", "v83", "v84", "v85", "v86", "v87", "v88", "v89", "v90", "v91", "v92", "v93", "v94", "v95", "v96", "v97", "v98", "v99", "v100", "v101", "v102", "v103", "v104", "v105", "v106", "v107", "v108", "v109", "v110", "v111", "v112", "v113", "v114", "v115", "v116", "v117", "v118", "v119");
    const float Sc = *(const float*)(smem + K2_SC + c * 4);
#pragma unroll
    for (int j = 0; j < 8; ++j) {
      const int t = 256 * j + 16 * i16 + 4 * g;
      *(u2v*)(smem + K2_YB + c * 4096 + t * 2) = u2v{pkbf(acc[j][0] * Sc, acc[j][1] * Sc), pkbf(acc[j][2] * Sc, acc[j][3] * Sc)};
    }
  }
  else {
    {
      __builtin_amdgcn_s_setprio(K2_HPRIO);
      const char* up = smem + K2_UL + c * 4608 + 2 * (256 + 16 * i16 - 24 + 8 * g);
      char* vq = smem + c * K2_CH + 32 * i16 + 16 * (g >> 1);
      const h8v band[4] = {__builtin_bit_cast(h8v, bandv[0]), __builtin_bit_cast(h8v, bandv[1]), __builtin_bit_cast(h8v, bandv[2]), __builtin_bit_cast(h8v, bandv[3])};
      h8v uw[7], u7[4];
#pragma unroll
      for (int j = 0; j < 7; ++j) uw[j] = *(const h8v*)(up + 512 * j);
#pragma unroll
      for (int j = 0; j < 4; ++j) u7[j] = *(const h8v*)(up + 512 * j + 16);
      f4v r3[7], r5[7], r7[4], r9[4];
      const f4v z4 = {0, 0, 0, 0};
#pragma unroll
      for (int j = 0; j < 7; ++j) {
        r3[j] = __builtin_amdgcn_mfma_f32_16x16x32_f16(band[0], uw[j], z4, 0, 0, 0);
        r5[j] = __builtin_amdgcn_mfma_f32_16x16x32_f16(band[1], uw[j], z4, 0, 0, 0);
        if (j < 4) {
          r7[j] = __builtin_amdgcn_mfma_f32_16x16x32_f16(band[2], u7[j], z4, 0, 0, 0);
          r9[j] = __builtin_amdgcn_mfma_f32_16x16x32_f16(band[3], uw[j], z4, 0, 0, 0);
        }
      }
      auto put2 = [&](f4v ra, f4v rb, int oa, int ob) {
        const auto s0 = __builtin_amdgcn_permlane16_swap(pkh(ra[0], ra[1]), pkh(rb[0], rb[1]), false, false);
        const auto s1 = __builtin_amdgcn_permlane16_swap(pkh(ra[2], ra[3]), pkh(rb[2], rb[3]), false, false);
        *(u4v*)(vq + ((g & 1) ? ob : oa)) = u4v{s0[0], s1[0], s0[1], s1[1]};
      };
#pragma unroll
      for (int j = 0; j < 7; ++j) {
        put2(r3[j], r5[j], K2_V3 + 512 * j, K2_V5 + 512 * j);
        if (j < 4) put2(r7[j], r9[j], K2_V7 + 512 * j, K2_V9 + 512 * j);
      }
      asm volatile("s_waitcnt lgkmcnt(0)" ::: "memory");
      if (lane == 0) *(volatile LDS_AS unsigned*)((LDS_AS char*)smem + K2_SC + 16 + c * 4) = 1u;
      __builtin_amdgcn_s_setprio(0);
    }
    const char* xt = ws + WS_XT + (size_t)(cg >> 1) * 32768 + (cg & 1) * 64 + (ht >> 2) * 128 + (ht & 3) * 16;
    u4v xv[4];
#pragma unroll
    for (int i = 0; i < 4; ++i) xv[i] = *(const u4v*)(xt + i * 8192);
#pragma unroll
    for (int i = 0; i < 4; ++i) *(u4v*)(smem + K2_XB + (ht & 3) * 4096 + (512 * i + 8 * (ht >> 2)) * 2) = xv[i];
  }
  __syncthreads();
  {
    const float dk[4] = {bflo(dsk[0]), bfhi(dsk[0]), bflo(dsk[1]), bfhi(dsk[1])};
#pragma unroll
    for (int hf = 0; hf < 2; ++hf) {
      const int t0 = 1024 * hf + 2 * tid;
      unsigned y[4], u[4];
#pragma unroll
      for (int cc = 0; cc < 4; ++cc) {
        y[cc] = *(const unsigned*)(smem + K2_YB + cc * 4096 + t0 * 2);
        u[cc] = *(const unsigned*)(smem + K2_UL + cc * 4608 + 512 + t0 * 2);
      }
      unsigned xq[4];
#pragma unroll
      for (int cc = 0; cc < 4; ++cc) xq[cc] = *(const unsigned*)(smem + K2_XB + cc * 4096 + t0 * 2);
      unsigned o[4];
#pragma unroll
      for (int i = 0; i < 2; ++i) {
        float hv[4];
#pragma unroll
        for (int cc = 0; cc < 4; cc += 2) {
          const float y0 = i ? bfhi(y[cc]) : bflo(y[cc]), y1 = i ? bfhi(y[cc + 1]) : bflo(y[cc + 1]);
          const float u0 = i ? hhi(u[cc]) : hlo(u[cc]), u1 = i ? hhi(u[cc + 1]) : hlo(u[cc + 1]);
          const unsigned ud = pkbf(u0 * dk[cc], u1 * dk[cc + 1]);
          const unsigned ss = pkbf(y0 + bflo(ud), y1 + bfhi(ud));
          hv[cc] = (i ? bfhi(xq[cc]) : bflo(xq[cc])) + bflo(ss);
          hv[cc + 1] = (i ? bfhi(xq[cc + 1]) : bflo(xq[cc + 1])) + bfhi(ss);
        }
        o[2 * i] = pkbf(hv[0], hv[1]);
        o[2 * i + 1] = pkbf(hv[2], hv[3]);
      }
      st16(AUX_H, ws + WS_H, (unsigned)((cg * SL + t0) * 8), u4v{o[0], o[1], o[2], o[3]});
    }
  }
}

constexpr int K3_WG = 16384, K3_WU = 16384 + 12288, K3_STAGE = 40960;
constexpr int K3_LDS = 4 * K3_STAGE;

DEV void glds16s(unsigned voff, const void* sbase, unsigned ldsdst) {
  asm volatile("s_mov_b32 m0, %2\n\ts_nop 0\n\tglobal_load_lds_dwordx4 %0, %1" ::"v"(voff), "s"(sbase), "s"(ldsdst) : "memory");
}

#define K3_MAINLOOP_ASM \
  "ds_read_b64_tr_b16 v[112:113], %[w0] offset:16384\n\t" \
  "ds_read_b64_tr_b16 v[114:115], %[w0] offset:19456\n\t" \
  "ds_read_b64 v[96:97], %[ta] offset:0\n\t" \
  "ds_read_b64 v[98:99], %[ta] offset:4096\n\t" \
  "ds_read_b64 v[100:101], %[tb] offset:0\n\t" \
  "ds_read_b64 v[102:103], %[tb] offset:4096\n\t" \
  "ds_read_b64 v[104:105], %[ta] offset:256\n\t" \
  "ds_read_b64 v[106:107], %[ta] offset:4352\n\t" \
  "ds_read_b64 v[108:109], %[tb] offset:256\n\t" \
  "ds_read_b64 v[110:111], %[tb] offset:4352\n\t" \
  "ds_read_b64_tr_b16 v[116:117], %[w0] offset:28672\n\t" \
  "ds_read_b64_tr_b16 v[118:119], %[w0] offset:31744\n\t" \
  "ds_read_b64_tr_b16 v[120:121], %[w1] offset:16384\n\t" \
  "ds_read_b64_tr_b16 v[122:123], %[w1] offset:19456\n\t" \
  "ds_read_b64_tr_b16 v[124:125], %[w1] offset:28672\n\t" \
  "ds_read_b64_tr_b16 v[126:127], %[w1] offset:31744\n\t" \
  "ds_read_b64_tr_b16 v[128:129], %[w2] offset:16384\n\t" \
  "ds_read_b64_tr_b16 v[130:131], %[w2] offset:19456\n\t" \
  "ds_read_b64_tr_b16 v[132:133], %[w2] offset:28672\n\t" \
  "ds_read_b64_tr_b16 v[134:135], %[w2] offset:31744\n\t" \
  "s_mov_b32 %[cnt], 10\n\t" \
  "s_mov_b32 %[so], 0\n\t" \
  "K3LOOP_%=:\n\t" \
  "s_waitcnt lgkmcnt(0)\n\t" \
  "v_mfma_f32_16x16x32_bf16 v[0:3], v[112:115], v[96:99], v[0:3]\n\t" \
  "ds_read_b64_tr_b16 v[152:153], %[w0] offset:22528\n\t" \
  "ds_read_b64_tr_b16 v[154:155], %[w0] offset:25600\n\t" \
  "v_mfma_f32_16x16x32_bf16 v[4:7], v[112:115], v[100:103], v[4:7]\n\t" \
  "ds_read_b64 v[136:137], %[ta] offset:8192\n\t" \
  "ds_read_b64 v[138:139], %[ta] offset:12288\n\t" \
  "v_mfma_f32_16x16x32_bf16 v[8:11], v[112:115], v[104:107], v[8:11]\n\t" \
  "ds_read_b64 v[140:141], %[tb] offset:8192\n\t" \
  "ds_read_b64 v[142:143], %[tb] offset:12288\n\t" \
  "v_mfma_f32_16x16x32_bf16 v[12:15], v[112:115], v[108:111], v[12:15]\n\t" \
  "ds_read_b64 v[144:145], %[ta] offset:8448\n\t" \
  "ds_read_b64 v[146:147], %[ta] offset:12544\n\t" \
  "v_mfma_f32_16x16x32_bf16 v[16:19], v[116:119], v[96:99], v[16:19]\n\t" \
  "ds_read_b64 v[148:149], %[tb] offset:8448\n\t" \
  "ds_read_b64 v[150:151], %[tb] offset:12544\n\t" \
  "v_mfma_f32_16x16x32_bf16 v[20:23], v[116:119], v[100:103], v[20:23]\n\t" \
  "ds_read_b64_tr_b16 v[156:157], %[w0] offset:34816\n\t" \
  "ds_read_b64_tr_b16 v[158:159], %[w0] offset:37888\n\t" \
  "v_mfma_f32_16x16x32_bf16 v[24:27], v[116:119], v[104:107], v[24:27]\n\t" \
  "ds_read_b64_tr_b16 v[160:161], %[w1] offset:22528\n\t" \
  "ds_read_b64_tr_b16 v[162:163], %[w1] offset:25600\n\t" \
  "v_mfma_f32_16x16x32_bf16 v[28:31], v[116:119], v[108:111], v[28:31]\n\t" \
  "ds_read_b64_tr_b16 v[164:165], %[w1] offset:34816\n\t" \
  "ds_read_b64_tr_b16 v[166:167], %[w1] offset:37888\n\t" \
  "v_mfma_f32_16x16x32_bf16 v[32:35], v[120:123], v[96:99], v[32:35]\n\t" \
  "ds_read_b64_tr_b16 v[168:169], %[w2] offset:22528\n\t" \
  "ds_read_b64_tr_b16 v[170:171], %[w2] offset:25600\n\t" \
  "v_mfma_f32_16x16x32_bf16 v[36:39], v[120:123], v[100:103], v[36:39]\n\t" \
  "ds_read_b64_tr_b16 v[172:173], %[w2] offset:34816\n\t" \
  "ds_read_b64_tr_b16 v[174:175], %[w2] offset:37888\n\t" \
  "v_mfma_f32_16x16x32_bf16 v[40:43], v[120:123], v[104:107], v[40:43]\n\t" \
  "v_mfma_f32_16x16x32_bf16 v[44:47], v[120:123], v[108:111], v[44:47]\n\t" \
  "v_mfma_f32_16x16x32_bf16 v[48:51], v[124:127], v[96:99], v[48:51]\n\t" \
  "v_mfma_f32_16x16x32_bf16 v[52:55], v[124:127], v[100:103], v[52:55]\n\t" \
  "v_mfma_f32_16x16x32_bf16 v[56:59], v[124:127], v[104:107], v[56:59]\n\t" \
  "v_mfma_f32_16x16x32_bf16 v[60:63], v[124:127], v[108:111], v[60:63]\n\t" \
  "v_mfma_f32_16x16x32_bf16 v[64:67], v[128:131], v[96:99], v[64:67]\n\t" \
  "v_mfma_f32_16x16x32_bf16 v[68:71], v[128:131], v[100:103], v[68:71]\n\t" \
  "v_mfma_f32_16x16x32_bf16 v[72:75], v[128:131], v[104:107], v[72:75]\n\t" \
  "v_mfma_f32_16x16x32_bf16 v[76:79], v[128:131], v[108:111], v[76:79]\n\t" \
  "v_mfma_f32_16x16x32_bf16 v[80:83], v[132:135], v[96:99], v[80:83]\n\t" \
  "v_mfma_f32_16x16x32_bf16 v[84:87], v[132:135], v[100:103], v[84:87]\n\t" \
  "v_mfma_f32_16x16x32_bf16 v[88:91], v[132:135], v[104:107], v[88:91]\n\t" \
  "v_mfma_f32_16x16x32_bf16 v[92:95], v[132:135], v[108:111], v[92:95]\n\t" \
  "s_add_u32 %[so], %[so], 40960\n\t" \
  "s_cmp_eq_u32 %[so], 163840\n\t" \
  "s_cselect_b32 %[so], 0, %[so]\n\t" \
  "v_add_u32 %[ta], %[so], %[tab]\n\t" \
  "v_add_u32 %[tb], %[so], %[tbb]\n\t" \
  "v_add_u32 %[w0], %[so], %[w0b]\n\t" \
  "v_add_u32 %[w1], %[so], %[w1b]\n\t" \
  "v_add_u32 %[w2], %[so], %[w2b]\n\t" \
  "s_waitcnt lgkmcnt(0)\n\t" \
  "v_mfma_f32_16x16x32_bf16 v[0:3], v[152:155], v[136:139], v[0:3]\n\t" \
  "s_barrier\n\t" \
  "ds_read_b64_tr_b16 v[112:113], %[w0] offset:16384\n\t" \
  "ds_read_b64_tr_b16 v[114:115], %[w0] offset:19456\n\t" \
  "v_mfma_f32_16x16x32_bf16 v[4:7], v[152:155], v[140:143], v[4:7]\n\t" \
  "ds_read_b64 v[96:97], %[ta] offset:0\n\t" \
  "ds_read_b64 v[98:99], %[ta] offset:4096\n\t" \
  "v_mfma_f32_16x16x32_bf16 v[8:11], v[152:155], v[144:147], v[8:11]\n\t" \
  "ds_read_b64 v[100:101], %[tb] offset:0\n\t" \
  "ds_read_b64 v[102:103], %[tb] offset:4096\n\t" \
  "v_mfma_f32_16x16x32_bf16 v[12:15], v[152:155], v[148:151], v[12:15]\n\t" \
  "ds_read_b64 v[104:105], %[ta] offset:256\n\t" \
  "ds_read_b64 v[106:107], %[ta] offset:4352\n\t" \
  "v_mfma_f32_16x16x32_bf16 v[16:19], v[156:159], v[136:139], v[16:19]\n\t" \
  "ds_read_b64 v[108:109], %[tb] offset:256\n\t" \
  "ds_read_b64 v[110:111], %[tb] offset:4352\n\t" \
  "v_mfma_f32_16x16x32_bf16 v[20:23], v[156:159], v[140:143], v[20:23]\n\t" \
  "ds_read_b64_tr_b16 v[116:117], %[w0] offset:28672\n\t" \
  "ds_read_b64_tr_b16 v[118:119], %[w0] offset:31744\n\t" \
  "v_mfma_f32_16x16x32_bf16 v[24:27], v[156:159], v[144:147], v[24:27]\n\t" \
  "ds_read_b64_tr_b16 v[120:121], %[w1] offset:16384\n\t" \
  "ds_read_b64_tr_b16 v[122:123], %[w1] offset:19456\n\t" \
  "v_mfma_f32_16x16x32_bf16 v[28:31], v[156:159], v[148:151], v[28:31]\n\t" \
  "ds_read_b64_tr_b16 v[124:125], %[w1] offset:28672\n\t" \
  "ds_read_b64_tr_b16 v[126:127], %[w1] offset:31744\n\t" \
  "v_mfma_f32_16x16x32_bf16 v[32:35], v[160:163], v[136:139], v[32:35]\n\t" \
  "ds_read_b64_tr_b16 v[128:129], %[w2] offset:16384\n\t" \
  "ds_read_b64_tr_b16 v[130:131], %[w2] offset:19456\n\t" \
  "v_mfma_f32_16x16x32_bf16 v[36:39], v[160:163], v[140:143], v[36:39]\n\t" \
  "ds_read_b64_tr_b16 v[132:133], %[w2] offset:28672\n\t" \
  "ds_read_b64_tr_b16 v[134:135], %[w2] offset:31744\n\t" \
  "v_mfma_f32_16x16x32_bf16 v[40:43], v[160:163], v[144:147], v[40:43]\n\t" \
  "v_mfma_f32_16x16x32_bf16 v[44:47], v[160:163], v[148:151], v[44:47]\n\t" \
  "v_mfma_f32_16x16x32_bf16 v[48:51], v[164:167], v[136:139], v[48:51]\n\t" \
  "v_mfma_f32_16x16x32_bf16 v[52:55], v[164:167], v[140:143], v[52:55]\n\t" \
  "v_mfma_f32_16x16x32_bf16 v[56:59], v[164:167], v[144:147], v[56:59]\n\t" \
  "v_mfma_f32_16x16x32_bf16 v[60:63], v[164:167], v[148:151], v[60:63]\n\t" \
  "v_mfma_f32_16x16x32_bf16 v[64:67], v[168:171], v[136:139], v[64:67]\n\t" \
  "v_mfma_f32_16x16x32_bf16 v[68:71], v[168:171], v[140:143], v[68:71]\n\t" \
  "v_mfma_f32_16x16x32_bf16 v[72:75], v[168:171], v[144:147], v[72:75]\n\t" \
  "v_mfma_f32_16x16x32_bf16 v[76:79], v[168:171], v[148:151], v[76:79]\n\t" \
  "v_mfma_f32_16x16x32_bf16 v[80:83], v[172:175], v[136:139], v[80:83]\n\t" \
  "v_mfma_f32_16x16x32_bf16 v[84:87], v[172:175], v[140:143], v[84:87]\n\t" \
  "v_mfma_f32_16x16x32_bf16 v[88:91], v[172:175], v[144:147], v[88:91]\n\t" \
  "v_mfma_f32_16x16x32_bf16 v[92:95], v[172:175], v[148:151], v[92:95]\n\t" \
  "s_sub_u32 %[cnt], %[cnt], 1\n\t" \
  "s_cmp_lg_u32 %[cnt], 0\n\t" \
  "s_cbranch_scc1 K3LOOP_%=\n\t" \
  "s_waitcnt lgkmcnt(0)\n\t" \
  "s_nop 7\n\t" \
  ""

#define K34B_ASM \
  "global_load_dwordx4 v[80:83], %[lo], s[46:47]\n\t" \
  "ds_read_b128 v[84:87], %[wl] offset:1024\n\t" \
  "ds_read_b128 v[88:91], %[wl] offset:2048\n\t" \
  "ds_read_b128 v[92:95], %[wl] offset:3072\n\t" \
  "ds_read_b128 v[96:99], %[wl] offset:4096\n\t" \
  "global_load_dwordx4 v[100:103], %[ho0], s[40:41] sc1\n\t" \
  "global_load_dwordx4 v[104:107], %[ho1], s[40:41] sc1\n\t" \
  "global_load_dwordx4 v[108:111], %[ho2], s[40:41] sc1\n\t" \
  "global_load_dwordx4 v[112:115], %[ho3], s[40:41] sc1\n\t" \
  "s_add_u32 s40, s40, 4096\n\t" \
  "s_addc_u32 s41, s41, 0\n\t" \
  "global_load_dwordx4 v[116:119], %[ho0], s[40:41] sc1\n\t" \
  "global_load_dwordx4 v[120:123], %[ho1], s[40:41] sc1\n\t" \
  "global_load_dwordx4 v[124:127], %[ho2], s[40:41] sc1\n\t" \
  "global_load_dwordx4 v[128:131], %[ho3], s[40:41] sc1\n\t" \
  "s_add_u32 s40, s40, 4096\n\t" \
  "s_addc_u32 s41, s41, 0\n\t" \
  "global_load_dwordx4 v[132:135], %[ho0], s[40:41] sc1\n\t" \
  "global_load_dwordx4 v[136:139], %[ho1], s[40:41] sc1\n\t" \
  "global_load_dwordx4 v[140:143], %[ho2], s[40:41] sc1\n\t" \
  "global_load_dwordx4 v[144:147], %[ho3], s[40:41] sc1\n\t" \
  "s_add_u32 s40, s40, 4096\n\t" \
  "s_addc_u32 s41, s41, 0\n\t" \
  "global_load_dwordx4 v[148:151], %[ho0], s[40:41] sc1\n\t" \
  "global_load_dwordx4 v[152:155], %[ho1], s[40:41] sc1\n\t" \
  "global_load_dwordx4 v[156:159], %[ho2], s[40:41] sc1\n\t" \
  "global_load_dwordx4 v[160:163], %[ho3], s[40:41] sc1\n\t" \
  "s_add_u32 s40, s40, 4096\n\t" \
  "s_addc_u32 s41, s41, 0\n\t" \
  "s_waitcnt vmcnt(16)\n\t" \
  "s_waitcnt vmcnt(15)\n\t" \
  "v_mfma_f32_16x16x32_bf16 v[0:3], v[80:83], v[100:103], v[0:3]\n\t" \
  "s_waitcnt vmcnt(14)\n\t" \
  "v_mfma_f32_16x16x32_bf16 v[20:23], v[80:83], v[104:107], v[20:23]\n\t" \
  "global_load_dwordx4 v[164:167], %[ho0], s[40:41] sc1\n\t" \
  "s_waitcnt vmcnt(14)\n\t" \
  "v_mfma_f32_16x16x32_bf16 v[40:43], v[80:83], v[108:111], v[40:43]\n\t" \
  "s_waitcnt vmcnt(13)\n\t" \
  "v_mfma_f32_16x16x32_bf16 v[60:63], v[80:83], v[112:115], v[60:63]\n\t" \
  "ds_read_b128 v[80:83], %[wl] offset:5120\n\t" \
  "s_add_u32 m0, %[wm], 0\n\t" \
  "s_add_u32 s42, s44, 0\n\t" \
  "s_addc_u32 s43, s45, 0\n\t" \
  "global_load_lds_dwordx4 %[lo], s[42:43]\n\t" \
  "s_waitcnt lgkmcnt(4)\n\t" \
  "v_mfma_f32_16x16x32_bf16 v[4:7], v[84:87], v[100:103], v[4:7]\n\t" \
  "v_mfma_f32_16x16x32_bf16 v[24:27], v[84:87], v[104:107], v[24:27]\n\t" \
  "global_load_dwordx4 v[168:171], %[ho1], s[40:41] sc1\n\t" \
  "v_mfma_f32_16x16x32_bf16 v[44:47], v[84:87], v[108:111], v[44:47]\n\t" \
  "v_mfma_f32_16x16x32_bf16 v[64:67], v[84:87], v[112:115], v[64:67]\n\t" \
  "ds_read_b128 v[84:87], %[wl] offset:6144\n\t" \
  "s_add_u32 m0, %[wm], 1024\n\t" \
  "s_add_u32 s42, s44, 1024\n\t" \
  "s_addc_u32 s43, s45, 0\n\t" \
  "global_load_lds_dwordx4 %[lo], s[42:43]\n\t" \
  "s_waitcnt lgkmcnt(4)\n\t" \
  "v_mfma_f32_16x16x32_bf16 v[8:11], v[88:91], v[100:103], v[8:11]\n\t" \
  "v_mfma_f32_16x16x32_bf16 v[28:31], v[88:91], v[104:107], v[28:31]\n\t" \
  "global_load_dwordx4 v[172:175], %[ho2], s[40:41] sc1\n\t" \
  "v_mfma_f32_16x16x32_bf16 v[48:51], v[88:91], v[108:111], v[48:51]\n\t" \
  "v_mfma_f32_16x16x32_bf16 v[68:71], v[88:91], v[112:115], v[68:71]\n\t" \
  "ds_read_b128 v[88:91], %[wl] offset:7168\n\t" \
  "s_add_u32 m0, %[wm], 2048\n\t" \
  "s_add_u32 s42, s44, 2048\n\t" \
  "s_addc_u32 s43, s45, 0\n\t" \
  "global_load_lds_dwordx4 %[lo], s[42:43]\n\t" \
  "s_waitcnt lgkmcnt(4)\n\t" \
  "v_mfma_f32_16x16x32_bf16 v[12:15], v[92:95], v[100:103], v[12:15]\n\t" \
  "v_mfma_f32_16x16x32_bf16 v[32:35], v[92:95], v[104:107], v[32:35]\n\t" \
  "global_load_dwordx4 v[176:179], %[ho3], s[40:41] sc1\n\t" \
  "s_add_u32 s40, s40, 4096\n\t" \
  "s_addc_u32 s41, s41, 0\n\t" \
  "v_mfma_f32_16x16x32_bf16 v[52:55], v[92:95], v[108:111], v[52:55]\n\t" \
  "v_mfma_f32_16x16x32_bf16 v[72:75], v[92:95], v[112:115], v[72:75]\n\t" \
  "ds_read_b128 v[92:95], %[wl] offset:8192\n\t" \
  "s_add_u32 m0, %[wm], 3072\n\t" \
  "s_add_u32 s42, s44, 3072\n\t" \
  "s_addc_u32 s43, s45, 0\n\t" \
  "global_load_lds_dwordx4 %[lo], s[42:43]\n\t" \
  "s_waitcnt lgkmcnt(4)\n\t" \
  "v_mfma_f32_16x16x32_bf16 v[16:19], v[96:99], v[100:103], v[16:19]\n\t" \
  "v_mfma_f32_16x16x32_bf16 v[36:39], v[96:99], v[104:107], v[36:39]\n\t" \
  "v_mfma_f32_16x16x32_bf16 v[56:59], v[96:99], v[108:111], v[56:59]\n\t" \
  "v_mfma_f32_16x16x32_bf16 v[76:79], v[96:99], v[112:115], v[76:79]\n\t" \
  "ds_read_b128 v[96:99], %[wl] offset:9216\n\t" \
  "s_add_u32 m0, %[wm], 4096\n\t" \
  "s_add_u32 s42, s44, 4096\n\t" \
  "s_addc_u32 s43, s45, 0\n\t" \
  "global_load_lds_dwordx4 %[lo], s[42:43]\n\t" \
  "s_waitcnt lgkmcnt(4)\n\t" \
  "s_waitcnt vmcnt(20)\n\t" \
  "v_mfma_f32_16x16x32_bf16 v[0:3], v[80:83], v[116:119], v[0:3]\n\t" \
  "s_waitcnt vmcnt(19)\n\t" \
  "v_mfma_f32_16x16x32_bf16 v[20:23], v[80:83], v[120:123], v[20:23]\n\t" \
  "global_load_dwordx4 v[100:103], %[ho0], s[40:41] sc1\n\t" \
  "s_waitcnt vmcnt(19)\n\t" \
  "v_mfma_f32_16x16x32_bf16 v[40:43], v[80:83], v[124:127], v[40:43]\n\t" \
  "s_waitcnt vmcnt(18)\n\t" \
  "v_mfma_f32_16x16x32_bf16 v[60:63], v[80:83], v[128:131], v[60:63]\n\t" \
  "ds_read_b128 v[80:83], %[wl] offset:10240\n\t" \
  "s_add_u32 m0, %[wm], 5120\n\t" \
  "s_add_u32 s42, s44, 5120\n\t" \
  "s_addc_u32 s43, s45, 0\n\t" \
  "global_load_lds_dwordx4 %[lo], s[42:43]\n\t" \
  "s_waitcnt lgkmcnt(4)\n\t" \
  "v_mfma_f32_16x16x32_bf16 v[4:7], v[84:87], v[116:119], v[4:7]\n\t" \
  "v_mfma_f32_16x16x32_bf16 v[24:27], v[84:87], v[120:123], v[24:27]\n\t" \
  "global_load_dwordx4 v[104:107], %[ho1], s[40:41] sc1\n\t" \
  "v_mfma_f32_16x16x32_bf16 v[44:47], v[84:87], v[124:127], v[44:47]\n\t" \
  "v_mfma_f32_16x16x32_bf16 v[64:67], v[84:87], v[128:131], v[64:67]\n\t" \
  "ds_read_b128 v[84:87], %[wl] offset:11264\n\t" \
  "s_add_u32 m0, %[wm], 6144\n\t" \
  "s_add_u32 s42, s44, 6144\n\t" \
  "s_addc_u32 s43, s45, 0\n\t" \
  "global_load_lds_dwordx4 %[lo], s[42:43]\n\t" \
  "s_waitcnt lgkmcnt(4)\n\t" \
  "v_mfma_f32_16x16x32_bf16 v[8:11], v[88:91], v[116:119], v[8:11]\n\t" \
  "v_mfma_f32_16x16x32_bf16 v[28:31], v[88:91], v[120:123], v[28:31]\n\t" \
  "global_load_dwordx4 v[108:111], %[ho2], s[40:41] sc1\n\t" \
  "v_mfma_f32_16x16x32_bf16 v[48:51], v[88:91], v[124:127], v[48:51]\n\t" \
  "v_mfma_f32_16x16x32_bf16 v[68:71], v[88:91], v[128:131], v[68:71]\n\t" \
  "ds_read_b128 v[88:91], %[wl] offset:12288\n\t" \
  "s_add_u32 m0, %[wm], 7168\n\t" \
  "s_add_u32 s42, s44, 7168\n\t" \
  "s_addc_u32 s43, s45, 0\n\t" \
  "global_load_lds_dwordx4 %[lo], s[42:43]\n\t" \
  "s_waitcnt lgkmcnt(4)\n\t" \
  "v_mfma_f32_16x16x32_bf16 v[12:15], v[92:95], v[116:119], v[12:15]\n\t" \
  "v_mfma_f32_16x16x32_bf16 v[32:35], v[92:95], v[120:123], v[32:35]\n\t" \
  "global_load_dwordx4 v[112:115], %[ho3], s[40:41] sc1\n\t" \
  "s_add_u32 s40, s40, 4096\n\t" \
  "s_addc_u32 s41, s41, 0\n\t" \
  "v_mfma_f32_16x16x32_bf16 v[52:55], v[92:95], v[124:127], v[52:55]\n\t" \
  "v_mfma_f32_16x16x32_bf16 v[72:75], v[92:95], v[128:131], v[72:75]\n\t" \
  "ds_read_b128 v[92:95], %[wl] offset:13312\n\t" \
  "s_add_u32 m0, %[wm], 8192\n\t" \
  "s_add_u32 s42, s44, 8192\n\t" \
  "s_addc_u32 s43, s45, 0\n\t" \
  "global_load_lds_dwordx4 %[lo], s[42:43]\n\t" \
  "s_waitcnt lgkmcnt(4)\n\t" \
  "v_mfma_f32_16x16x32_bf16 v[16:19], v[96:99], v[116:119], v[16:19]\n\t" \
  "v_mfma_f32_16x16x32_bf16 v[36:39], v[96:99], v[120:123], v[36:39]\n\t" \
  "v_mfma_f32_16x16x32_bf16 v[56:59], v[96:99], v[124:127], v[56:59]\n\t" \
  "v_mfma_f32_16x16x32_bf16 v[76:79], v[96:99], v[128:131], v[76:79]\n\t" \
  "ds_read_b128 v[96:99], %[wl] offset:14336\n\t" \
  "s_add_u32 m0, %[wm], 9216\n\t" \
  "s_add_u32 s42, s44, 9216\n\t" \
  "s_addc_u32 s43, s45, 0\n\t" \
  "global_load_lds_dwordx4 %[lo], s[42:43]\n\t" \
  "s_waitcnt lgkmcnt(4)\n\t" \
  "s_waitcnt vmcnt(25)\n\t" \
  "v_mfma_f32_16x16x32_bf16 v[0:3], v[80:83], v[132:135], v[0:3]\n\t" \
  "s_waitcnt vmcnt(24)\n\t" \
  "v_mfma_f32_16x16x32_bf16 v[20:23], v[80:83], v[136:139], v[20:23]\n\t" \
  "global_load_dwordx4 v[116:119], %[ho0], s[40:41] sc1\n\t" \
  "s_waitcnt vmcnt(24)\n\t" \
  "v_mfma_f32_16x16x32_bf16 v[40:43], v[80:83], v[140:143], v[40:43]\n\t" \
  "s_waitcnt vmcnt(23)\n\t" \
  "v_mfma_f32_16x16x32_bf16 v[60:63], v[80:83], v[144:147], v[60:63]\n\t" \
  "ds_read_b128 v[80:83], %[wl] offset:15360\n\t" \
  "s_add_u32 m0, %[wm], 10240\n\t" \
  "s_add_u32 s42, s44, 10240\n\t" \
  "s_addc_u32 s43, s45, 0\n\t" \
  "global_load_lds_dwordx4 %[lo], s[42:43]\n\t" \
  "s_waitcnt lgkmcnt(4)\n\t" \
  "v_mfma_f32_16x16x32_bf16 v[4:7], v[84:87], v[132:135], v[4:7]\n\t" \
  "v_mfma_f32_16x16x32_bf16 v[24:27], v[84:87], v[136:139], v[24:27]\n\t" \
  "global_load_dwordx4 v[120:123], %[ho1], s[40:41] sc1\n\t" \
  "v_mfma_f32_16x16x32_bf16 v[44:47], v[84:87], v[140:143], v[44:47]\n\t" \
  "v_mfma_f32_16x16x32_bf16 v[64:67], v[84:87], v[144:147], v[64:67]\n\t" \
  "ds_read_b128 v[84:87], %[wl] offset:16384\n\t" \
  "s_add_u32 m0, %[wm], 11264\n\t" \
  "s_add_u32 s42, s44, 11264\n\t" \
  "s_addc_u32 s43, s45, 0\n\t" \
  "global_load_lds_dwordx4 %[lo], s[42:43]\n\t" \
  "s_waitcnt lgkmcnt(4)\n\t" \
  "v_mfma_f32_16x16x32_bf16 v[8:11], v[88:91], v[132:135], v[8:11]\n\t" \
  "v_mfma_f32_16x16x32_bf16 v[28:31], v[88:91], v[136:139], v[28:31]\n\t" \
  "global_load_dwordx4 v[124:127], %[ho2], s[40:41] sc1\n\t" \
  "v_mfma_f32_16x16x32_bf16 v[48:51], v[88:91], v[140:143], v[48:51]\n\t" \
  "v_mfma_f32_16x16x32_bf16 v[68:71], v[88:91], v[144:147], v[68:71]\n\t" \
  "ds_read_b128 v[88:91], %[wl] offset:17408\n\t" \
  "s_add_u32 m0, %[wm], 12288\n\t" \
  "s_add_u32 s42, s44, 12288\n\t" \
  "s_addc_u32 s43, s45, 0\n\t" \
  "global_load_lds_dwordx4 %[lo], s[42:43]\n\t" \
  "s_waitcnt lgkmcnt(4)\n\t" \
  "v_mfma_f32_16x16x32_bf16 v[12:15], v[92:95], v[132:135], v[12:15]\n\t" \
  "v_mfma_f32_16x16x32_bf16 v[32:35], v[92:95], v[136:139], v[32:35]\n\t" \
  "global_load_dwordx4 v[128:131], %[ho3], s[40:41] sc1\n\t" \
  "s_add_u32 s40, s40, 4096\n\t" \
  "s_addc_u32 s41, s41, 0\n\t" \
  "v_mfma_f32_16x16x32_bf16 v[52:55], v[92:95], v[140:143], v[52:55]\n\t" \
  "v_mfma_f32_16x16x32_bf16 v[72:75], v[92:95], v[144:147], v[72:75]\n\t" \
  "ds_read_b128 v[92:95], %[wl] offset:18432\n\t" \
  "s_add_u32 m0, %[wm], 13312\n\t" \
  "s_add_u32 s42, s44, 13312\n\t" \
  "s_addc_u32 s43, s45, 0\n\t" \
  "global_load_lds_dwordx4 %[lo], s[42:43]\n\t" \
  "s_waitcnt lgkmcnt(4)\n\t" \
  "v_mfma_f32_16x16x32_bf16 v[16:19], v[96:99], v[132:135], v[16:19]\n\t" \
  "v_mfma_f32_16x16x32_bf16 v[36:39], v[96:99], v[136:139], v[36:39]\n\t" \
  "v_mfma_f32_16x16x32_bf16 v[56:59], v[96:99], v[140:143], v[56:59]\n\t" \
  "v_mfma_f32_16x16x32_bf16 v[76:79], v[96:99], v[144:147], v[76:79]\n\t" \
  "ds_read_b128 v[96:99], %[wl] offset:19456\n\t" \
  "s_add_u32 m0, %[wm], 14336\n\t" \
  "s_add_u32 s42, s44, 14336\n\t" \
  "s_addc_u32 s43, s45, 0\n\t" \
  "global_load_lds_dwordx4 %[lo], s[42:43]\n\t" \
  "s_waitcnt lgkmcnt(4)\n\t" \
  "s_waitcnt vmcnt(30)\n\t" \
  "v_mfma_f32_16x16x32_bf16 v[0:3], v[80:83], v[148:151], v[0:3]\n\t" \
  "s_waitcnt vmcnt(29)\n\t" \
  "v_mfma_f32_16x16x32_bf16 v[20:23], v[80:83], v[152:155], v[20:23]\n\t" \
  "global_load_dwordx4 v[132:135], %[ho0], s[40:41] sc1\n\t" \
  "s_waitcnt vmcnt(29)\n\t" \
  "v_mfma_f32_16x16x32_bf16 v[40:43], v[80:83], v[156:159], v[40:43]\n\t" \
  "s_waitcnt vmcnt(28)\n\t" \
  "v_mfma_f32_16x16x32_bf16 v[60:63], v[80:83], v[160:163], v[60:63]\n\t" \
  "ds_read_b128 v[80:83], %[wl] offset:20480\n\t" \
  "s_add_u32 m0, %[wm], 15360\n\t" \
  "s_add_u32 s42, s44, 15360\n\t" \
  "s_addc_u32 s43, s45, 0\n\t" \
  "global_load_lds_dwordx4 %[lo], s[42:43]\n\t" \
  "s_waitcnt lgkmcnt(4)\n\t" \
  "v_mfma_f32_16x16x32_bf16 v[4:7], v[84:87], v[148:151], v[4:7]\n\t" \
  "v_mfma_f32_16x16x32_bf16 v[24:27], v[84:87], v[152:155], v[24:27]\n\t" \
  "global_load_dwordx4 v[136:139], %[ho1], s[40:41] sc1\n\t" \
  "v_mfma_f32_16x16x32_bf16 v[44:47], v[84:87], v[156:159], v[44:47]\n\t" \
  "v_mfma_f32_16x16x32_bf16 v[64:67], v[84:87], v[160:163], v[64:67]\n\t" \
  "ds_read_b128 v[84:87], %[wl] offset:21504\n\t" \
  "s_add_u32 m0, %[wm], 16384\n\t" \
  "s_add_u32 s42, s44, 16384\n\t" \
  "s_addc_u32 s43, s45, 0\n\t" \
  "global_load_lds_dwordx4 %[lo], s[42:43]\n\t" \
  "s_waitcnt lgkmcnt(4)\n\t" \
  "v_mfma_f32_16x16x32_bf16 v[8:11], v[88:91], v[148:151], v[8:11]\n\t" \
  "v_mfma_f32_16x16x32_bf16 v[28:31], v[88:91], v[152:155], v[28:31]\n\t" \
  "global_load_dwordx4 v[140:143], %[ho2], s[40:41] sc1\n\t" \
  "v_mfma_f32_16x16x32_bf16 v[48:51], v[88:91], v[156:159], v[48:51]\n\t" \
  "v_mfma_f32_16x16x32_bf16 v[68:71], v[88:91], v[160:163], v[68:71]\n\t" \
  "ds_read_b128 v[88:91], %[wl] offset:22528\n\t" \
  "s_add_u32 m0, %[wm], 17408\n\t" \
  "s_add_u32 s42, s44, 17408\n\t" \
  "s_addc_u32 s43, s45, 0\n\t" \
  "global_load_lds_dwordx4 %[lo], s[42:43]\n\t" \
  "s_waitcnt lgkmcnt(4)\n\t" \
  "v_mfma_f32_16x16x32_bf16 v[12:15], v[92:95], v[148:151], v[12:15]\n\t" \
  "v_mfma_f32_16x16x32_bf16 v[32:35], v[92:95], v[152:155], v[32:35]\n\t" \
  "global_load_dwordx4 v[144:147], %[ho3], s[40:41] sc1\n\t" \
  "s_add_u32 s40, s40, 4096\n\t" \
  "s_addc_u32 s41, s41, 0\n\t" \
  "v_mfma_f32_16x16x32_bf16 v[52:55], v[92:95], v[156:159], v[52:55]\n\t" \
  "v_mfma_f32_16x16x32_bf16 v[72:75], v[92:95], v[160:163], v[72:75]\n\t" \
  "ds_read_b128 v[92:95], %[wl] offset:23552\n\t" \
  "s_add_u32 m0, %[wm], 18432\n\t" \
  "s_add_u32 s42, s44, 18432\n\t" \
  "s_addc_u32 s43, s45, 0\n\t" \
  "global_load_lds_dwordx4 %[lo], s[42:43]\n\t" \
  "s_waitcnt lgkmcnt(4)\n\t" \
  "v_mfma_f32_16x16x32_bf16 v[16:19], v[96:99], v[148:151], v[16:19]\n\t" \
  "v_mfma_f32_16x16x32_bf16 v[36:39], v[96:99], v[152:155], v[36:39]\n\t" \
  "v_mfma_f32_16x16x32_bf16 v[56:59], v[96:99], v[156:159], v[56:59]\n\t" \
  "v_mfma_f32_16x16x32_bf16 v[76:79], v[96:99], v[160:163], v[76:79]\n\t" \
  "ds_read_b128 v[96:99], %[wl] offset:24576\n\t" \
  "s_add_u32 m0, %[wm], 19456\n\t" \
  "s_add_u32 s42, s44, 19456\n\t" \
  "s_addc_u32 s43, s45, 0\n\t" \
  "global_load_lds_dwordx4 %[lo], s[42:43]\n\t" \
  "s_waitcnt lgkmcnt(4)\n\t" \
  "s_waitcnt vmcnt(35)\n\t" \
  "v_mfma_f32_16x16x32_bf16 v[0:3], v[80:83], v[164:167], v[0:3]\n\t" \
  "s_waitcnt vmcnt(33)\n\t" \
  "v_mfma_f32_16x16x32_bf16 v[20:23], v[80:83], v[168:171], v[20:23]\n\t" \
  "global_load_dwordx4 v[148:151], %[ho0], s[40:41] sc1\n\t" \
  "s_waitcnt vmcnt(32)\n\t" \
  "v_mfma_f32_16x16x32_bf16 v[40:43], v[80:83], v[172:175], v[40:43]\n\t" \
  "s_waitcnt vmcnt(30)\n\t" \
  "v_mfma_f32_16x16x32_bf16 v[60:63], v[80:83], v[176:179], v[60:63]\n\t" \
  "ds_read_b128 v[80:83], %[wl] offset:25600\n\t" \
  "s_waitcnt lgkmcnt(4)\n\t" \
  "v_mfma_f32_16x16x32_bf16 v[4:7], v[84:87], v[164:167], v[4:7]\n\t" \
  "v_mfma_f32_16x16x32_bf16 v[24:27], v[84:87], v[168:171], v[24:27]\n\t" \
  "global_load_dwordx4 v[152:155], %[ho1], s[40:41] sc1\n\t" \
  "v_mfma_f32_16x16x32_bf16 v[44:47], v[84:87], v[172:175], v[44:47]\n\t" \
  "v_mfma_f32_16x16x32_bf16 v[64:67], v[84:87], v[176:179], v[64:67]\n\t" \
  "ds_read_b128 v[84:87], %[wl] offset:26624\n\t" \
  "s_waitcnt lgkmcnt(4)\n\t" \
  "v_mfma_f32_16x16x32_bf16 v[8:11], v[88:91], v[164:167], v[8:11]\n\t" \
  "v_mfma_f32_16x16x32_bf16 v[28:31], v[88:91], v[168:171], v[28:31]\n\t" \
  "global_load_dwordx4 v[156:159], %[ho2], s[40:41] sc1\n\t" \
  "v_mfma_f32_16x16x32_bf16 v[48:51], v[88:91], v[172:175], v[48:51]\n\t" \
  "v_mfma_f32_16x16x32_bf16 v[68:71], v[88:91], v[176:179], v[68:71]\n\t" \
  "ds_read_b128 v[88:91], %[wl] offset:27648\n\t" \
  "s_waitcnt lgkmcnt(4)\n\t" \
  "v_mfma_f32_16x16x32_bf16 v[12:15], v[92:95], v[164:167], v[12:15]\n\t" \
  "v_mfma_f32_16x16x32_bf16 v[32:35], v[92:95], v[168:171], v[32:35]\n\t" \
  "global_load_dwordx4 v[160:163], %[ho3], s[40:41] sc1\n\t" \
  "s_add_u32 s40, s40, 4096\n\t" \
  "s_addc_u32 s41, s41, 0\n\t" \
  "v_mfma_f32_16x16x32_bf16 v[52:55], v[92:95], v[172:175], v[52:55]\n\t" \
  "v_mfma_f32_16x16x32_bf16 v[72:75], v[92:95], v[176:179], v[72:75]\n\t" \
  "ds_read_b128 v[92:95], %[wl] offset:28672\n\t" \
  "s_waitcnt lgkmcnt(4)\n\t" \
  "v_mfma_f32_16x16x32_bf16 v[16:19], v[96:99], v[164:167], v[16:19]\n\t" \
  "v_mfma_f32_16x16x32_bf16 v[36:39], v[96:99], v[168:171], v[36:39]\n\t" \
  "v_mfma_f32_16x16x32_bf16 v[56:59], v[96:99], v[172:175], v[56:59]\n\t" \
  "v_mfma_f32_16x16x32_bf16 v[76:79], v[96:99], v[176:179], v[76:79]\n\t" \
  "ds_read_b128 v[96:99], %[wl] offset:29696\n\t" \
  "s_waitcnt lgkmcnt(4)\n\t" \
  "s_waitcnt vmcnt(30)\n\t" \
  "v_mfma_f32_16x16x32_bf16 v[0:3], v[80:83], v[100:103], v[0:3]\n\t" \
  "s_waitcnt vmcnt(28)\n\t" \
  "v_mfma_f32_16x16x32_bf16 v[20:23], v[80:83], v[104:107], v[20:23]\n\t" \
  "global_load_dwordx4 v[164:167], %[ho0], s[40:41] sc1\n\t" \
  "s_waitcnt vmcnt(27)\n\t" \
  "v_mfma_f32_16x16x32_bf16 v[40:43], v[80:83], v[108:111], v[40:43]\n\t" \
  "s_waitcnt vmcnt(25)\n\t" \
  "v_mfma_f32_16x16x32_bf16 v[60:63], v[80:83], v[112:115], v[60:63]\n\t" \
  "ds_read_b128 v[80:83], %[wl] offset:30720\n\t" \
  "s_waitcnt lgkmcnt(4)\n\t" \
  "v_mfma_f32_16x16x32_bf16 v[4:7], v[84:87], v[100:103], v[4:7]\n\t" \
  "v_mfma_f32_16x16x32_bf16 v[24:27], v[84:87], v[104:107], v[24:27]\n\t" \
  "global_load_dwordx4 v[168:171], %[ho1], s[40:41] sc1\n\t" \
  "v_mfma_f32_16x16x32_bf16 v[44:47], v[84:87], v[108:111], v[44:47]\n\t" \
  "v_mfma_f32_16x16x32_bf16 v[64:67], v[84:87], v[112:115], v[64:67]\n\t" \
  "ds_read_b128 v[84:87], %[wl] offset:31744\n\t" \
  "s_waitcnt lgkmcnt(4)\n\t" \
  "v_mfma_f32_16x16x32_bf16 v[8:11], v[88:91], v[100:103], v[8:11]\n\t" \
  "v_mfma_f32_16x16x32_bf16 v[28:31], v[88:91], v[104:107], v[28:31]\n\t" \
  "global_load_dwordx4 v[172:175], %[ho2], s[40:41] sc1\n\t" \
  "v_mfma_f32_16x16x32_bf16 v[48:51], v[88:91], v[108:111], v[48:51]\n\t" \
  "v_mfma_f32_16x16x32_bf16 v[68:71], v[88:91], v[112:115], v[68:71]\n\t" \
  "ds_read_b128 v[88:91], %[wl] offset:32768\n\t" \
  "s_waitcnt lgkmcnt(4)\n\t" \
  "v_mfma_f32_16x16x32_bf16 v[12:15], v[92:95], v[100:103], v[12:15]\n\t" \
  "v_mfma_f32_16x16x32_bf16 v[32:35], v[92:95], v[104:107], v[32:35]\n\t" \
  "global_load_dwordx4 v[176:179], %[ho3], s[40:41] sc1\n\t" \
  "s_add_u32 s40, s40, 4096\n\t" \
  "s_addc_u32 s41, s41, 0\n\t" \
  "v_mfma_f32_16x16x32_bf16 v[52:55], v[92:95], v[108:111], v[52:55]\n\t" \
  "v_mfma_f32_16x16x32_bf16 v[72:75], v[92:95], v[112:115], v[72:75]\n\t" \
  "ds_read_b128 v[92:95], %[wl] offset:33792\n\t" \
  "s_waitcnt lgkmcnt(4)\n\t" \
  "v_mfma_f32_16x16x32_bf16 v[16:19], v[96:99], v[100:103], v[16:19]\n\t" \
  "v_mfma_f32_16x16x32_bf16 v[36:39], v[96:99], v[104:107], v[36:39]\n\t" \
  "v_mfma_f32_16x16x32_bf16 v[56:59], v[96:99], v[108:111], v[56:59]\n\t" \
  "v_mfma_f32_16x16x32_bf16 v[76:79], v[96:99], v[112:115], v[76:79]\n\t" \
  "ds_read_b128 v[96:99], %[wl] offset:34816\n\t" \
  "s_waitcnt lgkmcnt(4)\n\t" \
  "s_waitcnt vmcnt(25)\n\t" \
  "v_mfma_f32_16x16x32_bf16 v[0:3], v[80:83], v[116:119], v[0:3]\n\t" \
  "s_waitcnt vmcnt(23)\n\t" \
  "v_mfma_f32_16x16x32_bf16 v[20:23], v[80:83], v[120:123], v[20:23]\n\t" \
  "global_load_dwordx4 v[100:103], %[ho0], s[40:41] sc1\n\t" \
  "s_waitcnt vmcnt(22)\n\t" \
  "v_mfma_f32_16x16x32_bf16 v[40:43], v[80:83], v[124:127], v[40:43]\n\t" \
  "s_waitcnt vmcnt(20)\n\t" \
  "v_mfma_f32_16x16x32_bf16 v[60:63], v[80:83], v[128:131], v[60:63]\n\t" \
  "ds_read_b128 v[80:83], %[wl] offset:35840\n\t" \
  "s_waitcnt lgkmcnt(4)\n\t" \
  "v_mfma_f32_16x16x32_bf16 v[4:7], v[84:87], v[116:119], v[4:7]\n\t" \
  "v_mfma_f32_16x16x32_bf16 v[24:27], v[84:87], v[120:123], v[24:27]\n\t" \
  "global_load_dwordx4 v[104:107], %[ho1], s[40:41] sc1\n\t" \
  "v_mfma_f32_16x16x32_bf16 v[44:47], v[84:87], v[124:127], v[44:47]\n\t" \
  "v_mfma_f32_16x16x32_bf16 v[64:67], v[84:87], v[128:131], v[64:67]\n\t" \
  "ds_read_b128 v[84:87], %[wl] offset:36864\n\t" \
  "s_waitcnt lgkmcnt(4)\n\t" \
  "v_mfma_f32_16x16x32_bf16 v[8:11], v[88:91], v[116:119], v[8:11]\n\t" \
  "v_mfma_f32_16x16x32_bf16 v[28:31], v[88:91], v[120:123], v[28:31]\n\t" \
  "global_load_dwordx4 v[108:111], %[ho2], s[40:41] sc1\n\t" \
  "v_mfma_f32_16x16x32_bf16 v[48:51], v[88:91], v[124:127], v[48:51]\n\t" \
  "v_mfma_f32_16x16x32_bf16 v[68:71], v[88:91], v[128:131], v[68:71]\n\t" \
  "ds_read_b128 v[88:91], %[wl] offset:37888\n\t" \
  "s_waitcnt lgkmcnt(4)\n\t" \
  "v_mfma_f32_16x16x32_bf16 v[12:15], v[92:95], v[116:119], v[12:15]\n\t" \
  "v_mfma_f32_16x16x32_bf16 v[32:35], v[92:95], v[120:123], v[32:35]\n\t" \
  "global_load_dwordx4 v[112:115], %[ho3], s[40:41] sc1\n\t" \
  "s_add_u32 s40, s40, 4096\n\t" \
  "s_addc_u32 s41, s41, 0\n\t" \
  "v_mfma_f32_16x16x32_bf16 v[52:55], v[92:95], v[124:127], v[52:55]\n\t" \
  "v_mfma_f32_16x16x32_bf16 v[72:75], v[92:95], v[128:131], v[72:75]\n\t" \
  "ds_read_b128 v[92:95], %[wl] offset:38912\n\t" \
  "s_waitcnt lgkmcnt(4)\n\t" \
  "v_mfma_f32_16x16x32_bf16 v[16:19], v[96:99], v[116:119], v[16:19]\n\t" \
  "v_mfma_f32_16x16x32_bf16 v[36:39], v[96:99], v[120:123], v[36:39]\n\t" \
  "v_mfma_f32_16x16x32_bf16 v[56:59], v[96:99], v[124:127], v[56:59]\n\t" \
  "v_mfma_f32_16x16x32_bf16 v[76:79], v[96:99], v[128:131], v[76:79]\n\t" \
  "ds_read_b128 v[96:99], %[wl] offset:39936\n\t" \
  "s_waitcnt lgkmcnt(4)\n\t" \
  "s_waitcnt vmcnt(20)\n\t" \
  "v_mfma_f32_16x16x32_bf16 v[0:3], v[80:83], v[132:135], v[0:3]\n\t" \
  "s_waitcnt vmcnt(18)\n\t" \
  "v_mfma_f32_16x16x32_bf16 v[20:23], v[80:83], v[136:139], v[20:23]\n\t" \
  "global_load_dwordx4 v[116:119], %[ho0], s[40:41] sc1\n\t" \
  "s_waitcnt vmcnt(17)\n\t" \
  "v_mfma_f32_16x16x32_bf16 v[40:43], v[80:83], v[140:143], v[40:43]\n\t" \
  "s_waitcnt vmcnt(15)\n\t" \
  "v_mfma_f32_16x16x32_bf16 v[60:63], v[80:83], v[144:147], v[60:63]\n\t" \
  "ds_read_b128 v[80:83], %[wl] offset:0\n\t" \
  "s_waitcnt lgkmcnt(4)\n\t" \
  "v_mfma_f32_16x16x32_bf16 v[4:7], v[84:87], v[132:135], v[4:7]\n\t" \
  "v_mfma_f32_16x16x32_bf16 v[24:27], v[84:87], v[136:139], v[24:27]\n\t" \
  "global_load_dwordx4 v[120:123], %[ho1], s[40:41] sc1\n\t" \
  "v_mfma_f32_16x16x32_bf16 v[44:47], v[84:87], v[140:143], v[44:47]\n\t" \
  "v_mfma_f32_16x16x32_bf16 v[64:67], v[84:87], v[144:147], v[64:67]\n\t" \
  "ds_read_b128 v[84:87], %[wl] offset:1024\n\t" \
  "s_waitcnt lgkmcnt(4)\n\t" \
  "v_mfma_f32_16x16x32_bf16 v[8:11], v[88:91], v[132:135], v[8:11]\n\t" \
  "v_mfma_f32_16x16x32_bf16 v[28:31], v[88:91], v[136:139], v[28:31]\n\t" \
  "global_load_dwordx4 v[124:127], %[ho2], s[40:41] sc1\n\t" \
  "v_mfma_f32_16x16x32_bf16 v[48:51], v[88:91], v[140:143], v[48:51]\n\t" \
  "v_mfma_f32_16x16x32_bf16 v[68:71], v[88:91], v[144:147], v[68:71]\n\t" \
  "ds_read_b128 v[88:91], %[wl] offset:2048\n\t" \
  "s_waitcnt lgkmcnt(4)\n\t" \
  "v_mfma_f32_16x16x32_bf16 v[12:15], v[92:95], v[132:135], v[12:15]\n\t" \
  "v_mfma_f32_16x16x32_bf16 v[32:35], v[92:95], v[136:139], v[32:35]\n\t" \
  "global_load_dwordx4 v[128:131], %[ho3], s[40:41] sc1\n\t" \
  "s_add_u32 s40, s40, 4096\n\t" \
  "s_addc_u32 s41, s41, 0\n\t" \
  "v_mfma_f32_16x16x32_bf16 v[52:55], v[92:95], v[140:143], v[52:55]\n\t" \
  "v_mfma_f32_16x16x32_bf16 v[72:75], v[92:95], v[144:147], v[72:75]\n\t" \
  "ds_read_b128 v[92:95], %[wl] offset:3072\n\t" \
  "s_waitcnt lgkmcnt(4)\n\t" \
  "v_mfma_f32_16x16x32_bf16 v[16:19], v[96:99], v[132:135], v[16:19]\n\t" \
  "v_mfma_f32_16x16x32_bf16 v[36:39], v[96:99], v[136:139], v[36:39]\n\t" \
  "v_mfma_f32_16x16x32_bf16 v[56:59], v[96:99], v[140:143], v[56:59]\n\t" \
  "v_mfma_f32_16x16x32_bf16 v[76:79], v[96:99], v[144:147], v[76:79]\n\t" \
  "ds_read_b128 v[96:99], %[wl] offset:4096\n\t" \
  "s_waitcnt lgkmcnt(4)\n\t" \
  "s_waitcnt vmcnt(15)\n\t" \
  "v_mfma_f32_16x16x32_bf16 v[0:3], v[80:83], v[148:151], v[0:3]\n\t" \
  "s_waitcnt vmcnt(14)\n\t" \
  "v_mfma_f32_16x16x32_bf16 v[20:23], v[80:83], v[152:155], v[20:23]\n\t" \
  "s_waitcnt vmcnt(13)\n\t" \
  "v_mfma_f32_16x16x32_bf16 v[40:43], v[80:83], v[156:159], v[40:43]\n\t" \
  "s_waitcnt vmcnt(12)\n\t" \
  "v_mfma_f32_16x16x32_bf16 v[60:63], v[80:83], v[160:163], v[60:63]\n\t" \
  "ds_read_b128 v[80:83], %[wl] offset:5120\n\t" \
  "s_waitcnt lgkmcnt(4)\n\t" \
  "v_mfma_f32_16x16x32_bf16 v[4:7], v[84:87], v[148:151], v[4:7]\n\t" \
  "v_mfma_f32_16x16x32_bf16 v[24:27], v[84:87], v[152:155], v[24:27]\n\t" \
  "v_mfma_f32_16x16x32_bf16 v[44:47], v[84:87], v[156:159], v[44:47]\n\t" \
  "v_mfma_f32_16x16x32_bf16 v[64:67], v[84:87], v[160:163], v[64:67]\n\t" \
  "ds_read_b128 v[84:87], %[wl] offset:6144\n\t" \
  "s_waitcnt lgkmcnt(4)\n\t" \
  "v_mfma_f32_16x16x32_bf16 v[8:11], v[88:91], v[148:151], v[8:11]\n\t" \
  "v_mfma_f32_16x16x32_bf16 v[28:31], v[88:91], v[152:155], v[28:31]\n\t" \
  "v_mfma_f32_16x16x32_bf16 v[48:51], v[88:91], v[156:159], v[48:51]\n\t" \
  "v_mfma_f32_16x16x32_bf16 v[68:71], v[88:91], v[160:163], v[68:71]\n\t" \
  "ds_read_b128 v[88:91], %[wl] offset:7168\n\t" \
  "s_waitcnt lgkmcnt(4)\n\t" \
  "v_mfma_f32_16x16x32_bf16 v[12:15], v[92:95], v[148:151], v[12:15]\n\t" \
  "v_mfma_f32_16x16x32_bf16 v[32:35], v[92:95], v[152:155], v[32:35]\n\t" \
  "v_mfma_f32_16x16x32_bf16 v[52:55], v[92:95], v[156:159], v[52:55]\n\t" \
  "v_mfma_f32_16x16x32_bf16 v[72:75], v[92:95], v[160:163], v[72:75]\n\t" \
  "ds_read_b128 v[92:95], %[wl] offset:8192\n\t" \
  "s_waitcnt lgkmcnt(4)\n\t" \
  "v_mfma_f32_16x16x32_bf16 v[16:19], v[96:99], v[148:151], v[16:19]\n\t" \
  "v_mfma_f32_16x16x32_bf16 v[36:39], v[96:99], v[152:155], v[36:39]\n\t" \
  "v_mfma_f32_16x16x32_bf16 v[56:59], v[96:99], v[156:159], v[56:59]\n\t" \
  "v_mfma_f32_16x16x32_bf16 v[76:79], v[96:99], v[160:163], v[76:79]\n\t" \
  "ds_read_b128 v[96:99], %[wl] offset:9216\n\t" \
  "s_waitcnt lgkmcnt(4)\n\t" \
  "s_waitcnt vmcnt(11)\n\t" \
  "v_mfma_f32_16x16x32_bf16 v[0:3], v[80:83], v[164:167], v[0:3]\n\t" \
  "s_waitcnt vmcnt(10)\n\t" \
  "v_mfma_f32_16x16x32_bf16 v[20:23], v[80:83], v[168:171], v[20:23]\n\t" \
  "s_waitcnt vmcnt(9)\n\t" \
  "v_mfma_f32_16x16x32_bf16 v[40:43], v[80:83], v[172:175], v[40:43]\n\t" \
  "s_waitcnt vmcnt(8)\n\t" \
  "v_mfma_f32_16x16x32_bf16 v[60:63], v[80:83], v[176:179], v[60:63]\n\t" \
  "ds_read_b128 v[80:83], %[wl] offset:10240\n\t" \
  "s_waitcnt lgkmcnt(4)\n\t" \
  "v_mfma_f32_16x16x32_bf16 v[4:7], v[84:87], v[164:167], v[4:7]\n\t" \
  "v_mfma_f32_16x16x32_bf16 v[24:27], v[84:87], v[168:171], v[24:27]\n\t" \
  "v_mfma_f32_16x16x32_bf16 v[44:47], v[84:87], v[172:175], v[44:47]\n\t" \
  "v_mfma_f32_16x16x32_bf16 v[64:67], v[84:87], v[176:179], v[64:67]\n\t" \
  "ds_read_b128 v[84:87], %[wl] offset:11264\n\t" \
  "s_waitcnt lgkmcnt(4)\n\t" \
  "v_mfma_f32_16x16x32_bf16 v[8:11], v[88:91], v[164:167], v[8:11]\n\t" \
  "v_mfma_f32_16x16x32_bf16 v[28:31], v[88:91], v[168:171], v[28:31]\n\t" \
  "v_mfma_f32_16x16x32_bf16 v[48:51], v[88:91], v[172:175], v[48:51]\n\t" \
  "v_mfma_f32_16x16x32_bf16 v[68:71], v[88:91], v[176:179], v[68:71]\n\t" \
  "ds_read_b128 v[88:91], %[wl] offset:12288\n\t" \
  "s_waitcnt lgkmcnt(4)\n\t" \
  "v_mfma_f32_16x16x32_bf16 v[12:15], v[92:95], v[164:167], v[12:15]\n\t" \
  "v_mfma_f32_16x16x32_bf16 v[32:35], v[92:95], v[168:171], v[32:35]\n\t" \
  "v_mfma_f32_16x16x32_bf16 v[52:55], v[92:95], v[172:175], v[52:55]\n\t" \
  "v_mfma_f32_16x16x32_bf16 v[72:75], v[92:95], v[176:179], v[72:75]\n\t" \
  "ds_read_b128 v[92:95], %[wl] offset:13312\n\t" \
  "s_waitcnt lgkmcnt(4)\n\t" \
  "v_mfma_f32_16x16x32_bf16 v[16:19], v[96:99], v[164:167], v[16:19]\n\t" \
  "v_mfma_f32_16x16x32_bf16 v[36:39], v[96:99], v[168:171], v[36:39]\n\t" \
  "v_mfma_f32_16x16x32_bf16 v[56:59], v[96:99], v[172:175], v[56:59]\n\t" \
  "v_mfma_f32_16x16x32_bf16 v[76:79], v[96:99], v[176:179], v[76:79]\n\t" \
  "ds_read_b128 v[96:99], %[wl] offset:14336\n\t" \
  "s_waitcnt lgkmcnt(4)\n\t" \
  "s_waitcnt vmcnt(7)\n\t" \
  "v_mfma_f32_16x16x32_bf16 v[0:3], v[80:83], v[100:103], v[0:3]\n\t" \
  "s_waitcnt vmcnt(6)\n\t" \
  "v_mfma_f32_16x16x32_bf16 v[20:23], v[80:83], v[104:107], v[20:23]\n\t" \
  "s_waitcnt vmcnt(5)\n\t" \
  "v_mfma_f32_16x16x32_bf16 v[40:43], v[80:83], v[108:111], v[40:43]\n\t" \
  "s_waitcnt vmcnt(4)\n\t" \
  "v_mfma_f32_16x16x32_bf16 v[60:63], v[80:83], v[112:115], v[60:63]\n\t" \
  "ds_read_b128 v[80:83], %[wl] offset:15360\n\t" \
  "s_waitcnt lgkmcnt(4)\n\t" \
  "v_mfma_f32_16x16x32_bf16 v[4:7], v[84:87], v[100:103], v[4:7]\n\t" \
  "v_mfma_f32_16x16x32_bf16 v[24:27], v[84:87], v[104:107], v[24:27]\n\t" \
  "v_mfma_f32_16x16x32_bf16 v[44:47], v[84:87], v[108:111], v[44:47]\n\t" \
  "v_mfma_f32_16x16x32_bf16 v[64:67], v[84:87], v[112:115], v[64:67]\n\t" \
  "ds_read_b128 v[84:87], %[wl] offset:16384\n\t" \
  "s_waitcnt lgkmcnt(4)\n\t" \
  "v_mfma_f32_16x16x32_bf16 v[8:11], v[88:91], v[100:103], v[8:11]\n\t" \
  "v_mfma_f32_16x16x32_bf16 v[28:31], v[88:91], v[104:107], v[28:31]\n\t" \
  "v_mfma_f32_16x16x32_bf16 v[48:51], v[88:91], v[108:111], v[48:51]\n\t" \
  "v_mfma_f32_16x16x32_bf16 v[68:71], v[88:91], v[112:115], v[68:71]\n\t" \
  "ds_read_b128 v[88:91], %[wl] offset:17408\n\t" \
  "s_waitcnt lgkmcnt(4)\n\t" \
  "v_mfma_f32_16x16x32_bf16 v[12:15], v[92:95], v[100:103], v[12:15]\n\t" \
  "v_mfma_f32_16x16x32_bf16 v[32:35], v[92:95], v[104:107], v[32:35]\n\t" \
  "v_mfma_f32_16x16x32_bf16 v[52:55], v[92:95], v[108:111], v[52:55]\n\t" \
  "v_mfma_f32_16x16x32_bf16 v[72:75], v[92:95], v[112:115], v[72:75]\n\t" \
  "ds_read_b128 v[92:95], %[wl] offset:18432\n\t" \
  "s_waitcnt lgkmcnt(4)\n\t" \
  "v_mfma_f32_16x16x32_bf16 v[16:19], v[96:99], v[100:103], v[16:19]\n\t" \
  "v_mfma_f32_16x16x32_bf16 v[36:39], v[96:99], v[104:107], v[36:39]\n\t" \
  "v_mfma_f32_16x16x32_bf16 v[56:59], v[96:99], v[108:111], v[56:59]\n\t" \
  "v_mfma_f32_16x16x32_bf16 v[76:79], v[96:99], v[112:115], v[76:79]\n\t" \
  "ds_read_b128 v[96:99], %[wl] offset:19456\n\t" \
  "s_waitcnt lgkmcnt(4)\n\t" \
  "s_waitcnt vmcnt(3)\n\t" \
  "v_mfma_f32_16x16x32_bf16 v[0:3], v[80:83], v[116:119], v[0:3]\n\t" \
  "s_waitcnt vmcnt(2)\n\t" \
  "v_mfma_f32_16x16x32_bf16 v[20:23], v[80:83], v[120:123], v[20:23]\n\t" \
  "s_waitcnt vmcnt(1)\n\t" \
  "v_mfma_f32_16x16x32_bf16 v[40:43], v[80:83], v[124:127], v[40:43]\n\t" \
  "s_waitcnt vmcnt(0)\n\t" \
  "v_mfma_f32_16x16x32_bf16 v[60:63], v[80:83], v[128:131], v[60:63]\n\t" \
  "s_waitcnt lgkmcnt(3)\n\t" \
  "v_mfma_f32_16x16x32_bf16 v[4:7], v[84:87], v[116:119], v[4:7]\n\t" \
  "v_mfma_f32_16x16x32_bf16 v[24:27], v[84:87], v[120:123], v[24:27]\n\t" \
  "v_mfma_f32_16x16x32_bf16 v[44:47], v[84:87], v[124:127], v[44:47]\n\t" \
  "v_mfma_f32_16x16x32_bf16 v[64:67], v[84:87], v[128:131], v[64:67]\n\t" \
  "s_waitcnt lgkmcnt(2)\n\t" \
  "v_mfma_f32_16x16x32_bf16 v[8:11], v[88:91], v[116:119], v[8:11]\n\t" \
  "v_mfma_f32_16x16x32_bf16 v[28:31], v[88:91], v[120:123], v[28:31]\n\t" \
  "v_mfma_f32_16x16x32_bf16 v[48:51], v[88:91], v[124:127], v[48:51]\n\t" \
  "v_mfma_f32_16x16x32_bf16 v[68:71], v[88:91], v[128:131], v[68:71]\n\t" \
  "s_waitcnt lgkmcnt(1)\n\t" \
  "v_mfma_f32_16x16x32_bf16 v[12:15], v[92:95], v[116:119], v[12:15]\n\t" \
  "v_mfma_f32_16x16x32_bf16 v[32:35], v[92:95], v[120:123], v[32:35]\n\t" \
  "v_mfma_f32_16x16x32_bf16 v[52:55], v[92:95], v[124:127], v[52:55]\n\t" \
  "v_mfma_f32_16x16x32_bf16 v[72:75], v[92:95], v[128:131], v[72:75]\n\t" \
  "s_waitcnt lgkmcnt(0)\n\t" \
  "v_mfma_f32_16x16x32_bf16 v[16:19], v[96:99], v[116:119], v[16:19]\n\t" \
  "v_mfma_f32_16x16x32_bf16 v[36:39], v[96:99], v[120:123], v[36:39]\n\t" \
  "v_mfma_f32_16x16x32_bf16 v[56:59], v[96:99], v[124:127], v[56:59]\n\t" \
  "v_mfma_f32_16x16x32_bf16 v[76:79], v[96:99], v[128:131], v[76:79]\n\t" \
  "s_waitcnt vmcnt(0) lgkmcnt(0)\n\t" \
  "s_nop 7\n\t" \
  ""

__global__ __launch_bounds__(512) void k34_ffn(const u16* __restrict__ Wg, const u16* __restrict__ Wu, char* __restrict__ ws, u16* __restrict__ out) {
  extern __shared__ __attribute__((aligned(16))) char smem[];
  const int tid = threadIdx.x, w = __builtin_amdgcn_readfirstlane(tid >> 6), lane = tid & 63;
  const int xcd = blockIdx.x & 7, jj = blockIdx.x >> 3;
  const int rb = 8 * (xcd & 1) + (jj & 7), cb = 4 * (xcd >> 1) + (jj >> 3);
  const int rb64 = 2 * rb + ((xcd >> 1) & 1), cb80 = 4 * (xcd >> 2) + (jj >> 3);
  const int tok0 = 128 * rb, f0 = 96 * cb;
  if (w >= 4) {
    const int lw = w - 4;
    const char* hsrc = ws + WS_H + ((size_t)(4 * lw) * SL + tok0) * 8;
    const char* wsrc = (const char*)((lw >> 1) ? Wu : Wg) + f0 * 2;
    const unsigned sb = (unsigned)(size_t)(LDS_AS char*)smem;
    unsigned woff[6], hoff[4];
#pragma unroll
    for (int i = 0; i < 4; ++i) hoff[i] = ((lane * 16) ^ (128 * (i & 1))) + i * (SL * 8);
    auto issue_h = [&](int t, int slot) {
      const char* hs = hsrc + (size_t)t * (16 * SL * 8);
#pragma unroll
      for (int i = 0; i < 4; ++i) glds16s(hoff[i], hs, sb + slot * K3_STAGE + (4 * lw + i) * 1024);
    };
    issue_h(0, 0);
    __builtin_amdgcn_sched_barrier(0);
#pragma unroll
    for (int i = 0; i < 6; ++i) {
      const int ch = 384 * (lw & 1) + 64 * i + lane;
      const int r = (ch * 21846) >> 18, cp = ch - 12 * r;
      const int cc = cp ^ (((r >> 2) & 1) << 1);
      woff[i] = r * (FF * 2) + cc * 16;
    }
    const int wdst = ((lw >> 1) ? K3_WU : K3_WG) + 384 * (lw & 1) * 16;
    auto issue_w = [&](int t, int slot) {
      const char* wsx = wsrc + (size_t)t * (64 * FF * 2);
#pragma unroll
      for (int i = 0; i < 6; ++i) glds16s(woff[i], wsx, sb + slot * K3_STAGE + wdst + i * 1024);
    };
    auto issue = [&](int t, int slot) {
      issue_h(t, slot);
      issue_w(t, slot);
    };
    issue_w(0, 0);
    issue(1, 1);
    asm volatile("s_waitcnt vmcnt(10)" ::: "memory");
    __builtin_amdgcn_s_barrier();
    issue(2, 2); issue(3, 3);
    int slot = 0;
#pragma unroll 1
    for (int t = 0; t < 7; ++t) {
      asm volatile("s_waitcnt vmcnt(20)" ::: "memory");
      __builtin_amdgcn_s_barrier();
      if (t < 6) issue(t + 4, slot);
      slot = (slot + 1) & 3;
    }
    asm volatile("s_waitcnt vmcnt(10)" ::: "memory");
    __builtin_amdgcn_s_barrier();
    {
      const int tk = tok0 + 32 * lw + (lane & 31);
      unsigned long long g0 = 0;
      for (unsigned spin = 0; spin < (1u << 22); ++spin) {
        g0 = __hip_atomic_load((GLB_AS unsigned long long*)(ws + WS_ST + (size_t)tk * 8), __ATOMIC_RELAXED, __HIP_MEMORY_SCOPE_AGENT);
        if (__all((g0 & 0x80000000ull) != 0)) break;
      }
      if (lane < 32) *(u2v*)(smem + 2 * K3_STAGE + (32 * lw + lane) * 8) = u2v{(unsigned)g0 & 0x7fffffffu, (unsigned)(g0 >> 32)};
    }
    if (lw == 1 && lane == 0) *(unsigned*)(smem + 3 * K3_STAGE) = 0u;
    asm volatile("s_waitcnt vmcnt(0) lgkmcnt(0)" ::: "memory");
    __builtin_amdgcn_s_barrier();
    __builtin_amdgcn_s_barrier();
    {
      const char* wq = ws + WS_W2T + (size_t)(cb80 * 48 + 12 * lw) * 5120;
      const unsigned dst = sb + ((lw + 2) & 3) * K3_STAGE, voff = lane * 16;
#pragma unroll 1
      for (int f = 1; f < 40; ++f) glds16s(voff, wq + f * 1024, dst + f * 1024);
      asm volatile("s_waitcnt vmcnt(0)" ::: "memory");
    }
    __builtin_amdgcn_s_barrier();
    return;
  }
  const int wr = w & 1, wc = w >> 1;
  const unsigned sbase = (unsigned)(size_t)(LDS_AS char*)smem;
  const int g16 = lane >> 4, i16 = lane & 15, gp = g16 & 1;
  const unsigned tbase = sbase + g16 * 1024 + (64 * wr + i16) * 8;
  const unsigned tab = tbase + 128 * gp, tbb = tbase + 128 * (1 - gp);
  const unsigned wbase = sbase + (4 * g16 + ((lane >> 2) & 3)) * 192 + 8 * (lane & 3);
  const unsigned w0b = wbase + 32 * ((3 * wc + 0) ^ gp), w1b = wbase + 32 * ((3 * wc + 1) ^ gp), w2b = wbase + 32 * ((3 * wc + 2) ^ gp);
  {
    const int tk = tok0 + 8 * cb + 2 * w + (lane & 1), gq = lane >> 1;
    u2v hv[5];
#pragma unroll
    for (int i = 0; i < 5; ++i) hv[i] = *(const u2v*)(ws + WS_H + ((size_t)(gq + 32 * i) * SL + tk) * 8);
    float s1 = 0.f, s2 = 0.f;
#pragma unroll
    for (int i = 0; i < 5; ++i) {
      const float a0 = bflo(hv[i][0]), a1 = bfhi(hv[i][0]), a2 = bflo(hv[i][1]), a3 = bfhi(hv[i][1]);
      s1 += (a0 + a1) + (a2 + a3);
      s2 += (a0 * a0 + a1 * a1) + (a2 * a2 + a3 * a3);
    }
    s1 += dppf<0x128>(s1); s2 += dppf<0x128>(s2);
    s1 += dppf<0x124>(s1); s2 += dppf<0x124>(s2);
    s1 += dppf<0x122>(s1); s2 += dppf<0x122>(s2);
    s1 = rows_sum(s1); s2 = rows_sum(s2);
    const float mean = s1 * (1.f / DM);
    const float var = fmaxf(s2 * (1.f / DM) - mean * mean, 0.f);
    const float rstd = __builtin_amdgcn_rsqf(var + 1e-5f);
    const unsigned long long gr = ((unsigned long long)__builtin_bit_cast(unsigned, -mean) << 32) | (__builtin_bit_cast(unsigned, rstd) | 0x80000000u);
    if (lane < 2) __hip_atomic_store((GLB_AS unsigned long long*)(ws + WS_ST + (size_t)tk * 8), gr, __ATOMIC_RELAXED, __HIP_MEMORY_SCOPE_AGENT);
  }
  f4v csv[3][2];
  {
    const float* cs = (const float*)(ws + WS_CS) + f0 + 48 * wc + 4 * g16;
    const float* cu = cs + FF;
    asm volatile("global_load_dwordx4 %0, %6, off offset:0\n\t"
                 "global_load_dwordx4 %1, %7, off offset:0\n\t"
                 "global_load_dwordx4 %2, %6, off offset:64\n\t"
                 "global_load_dwordx4 %3, %7, off offset:64\n\t"
                 "global_load_dwordx4 %4, %6, off offset:128\n\t"
                 "global_load_dwordx4 %5, %7, off offset:128\n\t"
                 : "=&{v[176:179]}"(csv[0][0]), "=&{v[180:183]}"(csv[0][1]), "=&{v[184:187]}"(csv[1][0]), "=&{v[188:191]}"(csv[1][1]), "=&{v[192:195]}"(csv[2][0]), "=&{v[196:199]}"(csv[2][1])
                 : "v"(cs), "v"(cu) : "memory");
  }
  f4v acc[3][2][4];
#pragma unroll
  for (int a = 0; a < 3; ++a)
#pragma unroll
    for (int b = 0; b < 2; ++b)
#pragma unroll
      for (int r = 0; r < 4; ++r) acc[a][b][r] = f4v{0, 0, 0, 0};
  asm volatile("" : "+{v[0:3]}"(acc[0][0][0]), "+{v[4:7]}"(acc[0][0][1]), "+{v[8:11]}"(acc[0][0][2]), "+{v[12:15]}"(acc[0][0][3]), "+{v[16:19]}"(acc[0][1][0]), "+{v[20:23]}"(acc[0][1][1]), "+{v[24:27]}"(acc[0][1][2]), "+{v[28:31]}"(acc[0][1][3]), "+{v[32:35]}"(acc[1][0][0]), "+{v[36:39]}"(acc[1][0][1]), "+{v[40:43]}"(acc[1][0][2]), "+{v[44:47]}"(acc[1][0][3]), "+{v[48:51]}"(acc[1][1][0]), "+{v[52:55]}"(acc[1][1][1]), "+{v[56:59]}"(acc[1][1][2]), "+{v[60:63]}"(acc[1][1][3]), "+{v[64:67]}"(acc[2][0][0]), "+{v[68:71]}"(acc[2][0][1]), "+{v[72:75]}"(acc[2][0][2]), "+{v[76:79]}"(acc[2][0][3]), "+{v[80:83]}"(acc[2][1][0]), "+{v[84:87]}"(acc[2][1][1]), "+{v[88:91]}"(acc[2][1][2]), "+{v[92:95]}"(acc[2][1][3]));
  __builtin_amdgcn_s_barrier();
  CFENCE();
  {
    unsigned ta = tab, tb = tbb, w0 = w0b, w1 = w1b, w2 = w2b, cnt, so;
    asm volatile(K3_MAINLOOP_ASM
                 : "+{v[0:3]}"(acc[0][0][0]), "+{v[4:7]}"(acc[0][0][1]), "+{v[8:11]}"(acc[0][0][2]), "+{v[12:15]}"(acc[0][0][3]), "+{v[16:19]}"(acc[0][1][0]), "+{v[20:23]}"(acc[0][1][1]), "+{v[24:27]}"(acc[0][1][2]), "+{v[28:31]}"(acc[0][1][3]), "+{v[32:35]}"(acc[1][0][0]), "+{v[36:39]}"(acc[1][0][1]), "+{v[40:43]}"(acc[1][0][2]), "+{v[44:47]}"(acc[1][0][3]), "+{v[48:51]}"(acc[1][1][0]), "+{v[52:55]}"(acc[1][1][1]), "+{v[56:59]}"(acc[1][1][2]), "+{v[60:63]}"(acc[1][1][3]), "+{v[64:67]}"(acc[2][0][0]), "+{v[68:71]}"(acc[2][0][1]), "+{v[72:75]}"(acc[2][0][2]), "+{v[76:79]}"(acc[2][0][3]), "+{v[80:83]}"(acc[2][1][0]), "+{v[84:87]}"(acc[2][1][1]), "+{v[88:91]}"(acc[2][1][2]), "+{v[92:95]}"(acc[2][1][3]),
                   [ta] "+&v"(ta), [tb] "+&v"(tb), [w0] "+&v"(w0), [w1] "+&v"(w1), [w2] "+&v"(w2), [cnt] "=&s"(cnt), [so] "=&s"(so)
                 : [tab] "v"(tab), [tbb] "v"(tbb), [w0b] "v"(w0b), [w1b] "v"(w1b), [w2b] "v"(w2b)
                 : "memory", "scc", "v96", "v97", "v98", "v99", "v100", "v101", "v102", "v103", "v104", "v105", "v106", "v107", "v108", "v109", "v110", "v111", "v112", "v113", "v114", "v115", "v116", "v117", "v118", "v119", "v120", "v121", "v122", "v123", "v124", "v125", "v126", "v127", "v128", "v129", "v130", "v131", "v132", "v133", "v134", "v135", "v136", "v137", "v138", "v139", "v140", "v141", "v142", "v143", "v144", "v145", "v146", "v147", "v148", "v149", "v150", "v151", "v152", "v153", "v154", "v155", "v156", "v157", "v158", "v159", "v160", "v161", "v162", "v163", "v164", "v165", "v166", "v167", "v168", "v169", "v170", "v171", "v172", "v173", "v174", "v175");
  }
  asm volatile("s_waitcnt vmcnt(0)" : "+{v[176:179]}"(csv[0][0]), "+{v[180:183]}"(csv[0][1]), "+{v[184:187]}"(csv[1][0]), "+{v[188:191]}"(csv[1][1]), "+{v[192:195]}"(csv[2][0]), "+{v[196:199]}"(csv[2][1]) : : "memory");
  f2v stv[4];
#pragma unroll
  for (int tt = 0; tt < 4; ++tt) stv[tt] = *(const f2v*)(smem + 2 * K3_STAGE + (64 * wr + 16 * tt + i16) * 8);
#pragma unroll
  for (int a = 0; a < 3; ++a) {
    const f4v cg = csv[a][0], cu = csv[a][1];
#pragma unroll
    for (int tp = 0; tp < 4; tp += 2) {
      unsigned ov[2][2];
#pragma unroll
      for (int b = 0; b < 2; ++b) {
        const f2v st = stv[tp + b];
        const f2v rs2 = {st[0], st[0]}, nm2 = {st[1], st[1]};
#pragma unroll
        for (int r = 0; r < 2; ++r) {
          const f2v cg2 = {cg[2 * r], cg[2 * r + 1]}, cu2 = {cu[2 * r], cu[2 * r + 1]};
          const f4v ag4 = acc[a][0][tp + b], au4 = acc[a][1][tp + b];
          const f2v ag = {ag4[2 * r], ag4[2 * r + 1]}, au = {au4[2 * r], au4[2 * r + 1]};
          const f2v gg = (ag + nm2 * cg2) * rs2;
          const f2v u = (au + nm2 * cu2) * rs2;
          const f2v y = gg * (gg * gg * -0.1029432397f + -2.302208198f);
          const f2v dd = f2v{__builtin_amdgcn_exp2f(y[0]), __builtin_amdgcn_exp2f(y[1])} + 1.f;
          const f2v rc = {__builtin_amdgcn_rcpf(dd[0]), __builtin_amdgcn_rcpf(dd[1])};
          const f2v hv = (gg * u) * rc;
          ov[b][r] = pkbf(hv[0], hv[1]);
        }
      }
      const auto s0 = __builtin_amdgcn_permlane16_swap(ov[0][0], ov[1][0], false, false);
      const auto s1 = __builtin_amdgcn_permlane16_swap(ov[0][1], ov[1][1], false, false);
      const int tok = tok0 + 64 * wr + 16 * (tp + gp) + i16;
      const int ftg = 6 * cb + 3 * wc + a;
      const int ks = ftg >> 1, g4 = 2 * (ftg & 1) + (g16 >> 1);
      st16(AUX_HID, ws + WS_HID, (unsigned)(((((tok >> 6) * 48 + ks) * 4 + ((tok >> 4) & 3)) * 64 + 16 * g4 + (tok & 15)) * 16), u4v{s0[0], s1[0], s0[1], s1[1]});
    }
  }
  asm volatile("s_waitcnt vmcnt(0)" ::: "memory");
  if (lane == 0) {
    const unsigned old = __hip_atomic_fetch_add((LDS_AS unsigned*)(smem + 3 * K3_STAGE), 1u, __ATOMIC_RELAXED, __HIP_MEMORY_SCOPE_WORKGROUP);
    if (old == 3u) __hip_atomic_fetch_add((GLB_AS unsigned*)(ws + WS_CNT + (4 * rb + (cb >> 2)) * 128), 1u, __ATOMIC_RELAXED, __HIP_MEMORY_SCOPE_AGENT);
  }
  const int g = lane >> 4, tok = 64 * rb64 + 16 * w + (lane & 15);
  u2v res[5];
#pragma unroll
  for (int i = 0; i < 5; ++i) res[i] = *(const u2v*)(ws + WS_H + ((size_t)(20 * cb80 + 4 * i + g) * SL + tok) * 8);
  f4v ac2[4][5];
#pragma unroll
  for (int a = 0; a < 4; ++a)
#pragma unroll
    for (int b = 0; b < 5; ++b) ac2[a][b] = f4v{0, 0, 0, 0};
  const unsigned slot = sbase + ((w + 2) & 3) * K3_STAGE;
  unsigned ho0 = lane * 16 + ((0 + w) & 3) * 1024, ho1 = lane * 16 + ((1 + w) & 3) * 1024, ho2 = lane * 16 + ((2 + w) & 3) * 1024,
           ho3 = lane * 16 + ((3 + w) & 3) * 1024, lo = lane * 16, wl = slot + lane * 16;
  unsigned long long hb = (unsigned long long)(ws + WS_HID + (size_t)(rb64 * 48 + 12 * w) * 4096);
  const unsigned long long w0 = (unsigned long long)(ws + WS_W2T + (size_t)(cb80 * 48 + 12 * w) * 5120), wb = w0 + 8 * 5120;
  asm volatile("" : "+{v[0:3]}"(ac2[0][0]), "+{v[4:7]}"(ac2[0][1]), "+{v[8:11]}"(ac2[0][2]), "+{v[12:15]}"(ac2[0][3]), "+{v[16:19]}"(ac2[0][4]), "+{v[20:23]}"(ac2[1][0]), "+{v[24:27]}"(ac2[1][1]), "+{v[28:31]}"(ac2[1][2]), "+{v[32:35]}"(ac2[1][3]), "+{v[36:39]}"(ac2[1][4]), "+{v[40:43]}"(ac2[2][0]), "+{v[44:47]}"(ac2[2][1]), "+{v[48:51]}"(ac2[2][2]), "+{v[52:55]}"(ac2[2][3]), "+{v[56:59]}"(ac2[2][4]), "+{v[60:63]}"(ac2[3][0]), "+{v[64:67]}"(ac2[3][1]), "+{v[68:71]}"(ac2[3][2]), "+{v[72:75]}"(ac2[3][3]), "+{v[76:79]}"(ac2[3][4]), "+v"(ho0), "+v"(ho1), "+v"(ho2), "+v"(ho3), "+v"(lo), "+v"(wl));
  __builtin_amdgcn_s_barrier();
  for (unsigned spin = 0; spin < (1u << 22); ++spin) {
    const unsigned cnt = __hip_atomic_load((GLB_AS unsigned*)(ws + WS_CNT + (4 * rb + w) * 128), __ATOMIC_RELAXED, __HIP_MEMORY_SCOPE_AGENT);
    if (__builtin_amdgcn_readfirstlane(cnt) >= 4u) break;
    __builtin_amdgcn_s_sleep(1);
  }
  CFENCE();
  asm volatile(K34B_ASM
               : "+{v[0:3]}"(ac2[0][0]), "+{v[4:7]}"(ac2[0][1]), "+{v[8:11]}"(ac2[0][2]), "+{v[12:15]}"(ac2[0][3]), "+{v[16:19]}"(ac2[0][4]), "+{v[20:23]}"(ac2[1][0]), "+{v[24:27]}"(ac2[1][1]), "+{v[28:31]}"(ac2[1][2]), "+{v[32:35]}"(ac2[1][3]), "+{v[36:39]}"(ac2[1][4]), "+{v[40:43]}"(ac2[2][0]), "+{v[44:47]}"(ac2[2][1]), "+{v[48:51]}"(ac2[2][2]), "+{v[52:55]}"(ac2[2][3]), "+{v[56:59]}"(ac2[2][4]), "+{v[60:63]}"(ac2[3][0]), "+{v[64:67]}"(ac2[3][1]), "+{v[68:71]}"(ac2[3][2]), "+{v[72:75]}"(ac2[3][3]), "+{v[76:79]}"(ac2[3][4]), "+{s[40:41]}"(hb)
               : [ho0] "v"(ho0), [ho1] "v"(ho1), [ho2] "v"(ho2), [ho3] "v"(ho3), [lo] "v"(lo), [wl] "v"(wl), [wm] "s"(slot),
                 "{s[44:45]}"(wb), "{s[46:47]}"(w0)
               : "memory", "scc", "s42", "s43", "v80", "v81", "v82", "v83", "v84", "v85", "v86", "v87", "v88", "v89", "v90", "v91", "v92", "v93", "v94", "v95", "v96", "v97", "v98", "v99", "v100", "v101", "v102", "v103", "v104", "v105", "v106", "v107", "v108", "v109", "v110", "v111", "v112", "v113", "v114", "v115", "v116", "v117", "v118", "v119", "v120", "v121", "v122", "v123", "v124", "v125", "v126", "v127", "v128", "v129", "v130", "v131", "v132", "v133", "v134", "v135", "v136", "v137", "v138", "v139", "v140", "v141", "v142", "v143", "v144", "v145", "v146", "v147", "v148", "v149", "v150", "v151", "v152", "v153", "v154", "v155", "v156", "v157", "v158", "v159", "v160", "v161", "v162", "v163", "v164", "v165", "v166", "v167", "v168", "v169", "v170", "v171", "v172", "v173", "v174", "v175", "v176", "v177", "v178", "v179", "v180", "v181", "v182", "v183", "v184", "v185", "v186", "v187", "v188", "v189", "v190", "v191", "v192", "v193", "v194", "v195", "v196", "v197", "v198", "v199", "v200", "v201", "v202", "v203", "v204", "v205", "v206", "v207", "v208", "v209", "v210", "v211", "v212", "v213", "v214", "v215", "v216", "v217", "v218", "v219", "v220", "v221", "v222", "v223", "v224", "v225", "v226", "v227");
  {
    f4v* mine = (f4v*)(smem + ((w + 2) & 3) * K3_STAGE);
#pragma unroll
    for (int j = 1; j < 4; ++j)
#pragma unroll
      for (int b = 0; b < 5; ++b) mine[((j - 1) * 5 + b) * 64 + lane] = ac2[j][b];
  }
  asm volatile("s_waitcnt lgkmcnt(0)" ::: "memory");
  __builtin_amdgcn_s_barrier();
  CFENCE();
#pragma unroll
  for (int b = 0; b < 5; ++b) {
    f4v v = ac2[0][b];
#pragma unroll
    for (int j = 1; j < 4; ++j)
      v += *(const f4v*)(smem + ((w - j + 2) & 3) * K3_STAGE + (((j - 1) * 5 + b) * 64 + lane) * 16);
    const unsigned o01 = pkbf(v[0], v[1]), o23 = pkbf(v[2], v[3]);
    const unsigned r0 = res[b][0], r1 = res[b][1];
    *(u2v*)(out + (size_t)tok * DM + 80 * cb80 + 16 * b + 4 * g) = u2v{pkbf(bflo(r0) + bflo(o01), bfhi(r0) + bfhi(o01)), pkbf(bflo(r1) + bflo(o23), bfhi(r1) + bfhi(o23))};
  }
}

extern "C" void kernel_launch(void* const* d_in, const int* in_sizes, int n_in, void* d_out, int out_size, void* d_ws, size_t ws_size,
                              hipStream_t stream) {
  const u16* x = (const u16*)d_in[0];
  const u16* logA = (const u16*)d_in[1];
  const u16* Aim = (const u16*)d_in[2];
  const u16* Cre = (const u16*)d_in[3];
  const u16* Cim = (const u16*)d_in[4];
  const u16* logdt = (const u16*)d_in[5];
  const u16* Dsk = (const u16*)d_in[6];
  const u16* Wg = (const u16*)d_in[8];
  const u16* Wu = (const u16*)d_in[9];
  const u16* Wd = (const u16*)d_in[10];
  char* ws = (char*)d_ws;
  (void)hipFuncSetAttribute((const void*)k2_conv, hipFuncAttributeMaxDynamicSharedMemorySize, K2_LDS);
  (void)hipFuncSetAttribute((const void*)k34_ffn, hipFuncAttributeMaxDynamicSharedMemorySize, K3_LDS);
  k1_ln_kgen<<<K1_NLN + K1_NKG + 128, 128, 0, stream>>>(x, logA, Aim, Cre, Cim, logdt, (const char*)Wg, (const char*)Wu, (const char*)Wd, ws);
  k2_conv<<<256, 512, K2_LDS, stream>>>(Dsk, Wg, Wu, Wd, ws);
  k34_ffn<<<256, 512, K3_LDS, stream>>>(Wg, Wu, ws, (u16*)d_out);
}
```
